```python
import torch

def _softmax(x, axis=-1):
    return torch.softmax(x, dim=axis)

def _gelu(x):
    return torch.nn.functional.gelu(x, approximate='tanh')

def _silu(x):
    return torch.nn.functional.silu(x)

def _sigmoid(x):
    return torch.sigmoid(x)

def _layernorm(x, axis=-1, eps=1e-05):
    m = x.mean(dim=axis, keepdim=True)
    v = x.var(dim=axis, keepdim=True, unbiased=False)
    return (x - m) / torch.sqrt(v + eps)

def _scatter_add(tgt, idx, src, axis=0):
    return tgt.clone().index_add_(axis, idx, src)

def _segment_sum(src, seg, n):
    out = torch.zeros((n,) + src.shape[1:], dtype=src.dtype)
    return out.index_add_(0, seg, src)

def _pad(x, pw):
    flat = tuple((v for pair in reversed(pw) for v in pair))
    return torch.nn.functional.pad(x, flat)
kernel_name = 'adaLN_cond__linear_kernel_hedgehog__glu_gated_swiglu__prompt'

def setup_inputs():

    _w = lambda a: (lambda t: t.to(torch.bfloat16) if t.is_floating_point() else t)(torch.as_tensor(a))

    def _A_setup():
        B, S, D, H = (1, 2048, 1024, 32)
        Dh = D // H
        return {'x': _w((lambda *s: torch.randn(*s, dtype=torch.bfloat16) * 0.05)(B, S, D)), 'wqkv': _w((lambda *s: torch.randn(*s, dtype=torch.bfloat16) * 0.4)(D, 3 * D)), 'w_phi': _w((lambda *s: torch.randn(*s, dtype=torch.bfloat16) * 0.4)(H, Dh, Dh) * Dh ** (-0.5)), 'wo': _w((lambda *s: torch.randn(*s, dtype=torch.bfloat16) * 0.1)(D, D))}
    _A_in = _A_setup()

    def _B_setup():
        B, S, D, F = (1, 2048, 1024, 3584)
        return {'x': _w((lambda *s: torch.randn(*s, dtype=torch.bfloat16) * 0.05)(B, S, D)), 'w1': _w((lambda *s: torch.randn(*s, dtype=torch.bfloat16) * 1.6)(D, F) * (2.0 / D) ** 0.5), 'w3': _w((lambda *s: torch.randn(*s, dtype=torch.bfloat16) * 1.6)(D, F) * (2.0 / D) ** 0.5), 'w2': _w((lambda *s: torch.randn(*s, dtype=torch.bfloat16) * 1.6)(F, D) * (2.0 / F) ** 0.5)}
    _B_in = _B_setup()
    return {'A_x': _A_in['x'], 'A_wqkv': _A_in['wqkv'], 'A_w_phi': _A_in['w_phi'], 'A_wo': _A_in['wo'], 'B_x': _B_in['x'], 'B_w1': _B_in['w1'], 'B_w3': _B_in['w3'], 'B_w2': _B_in['w2'], 'c': _w((lambda *s: torch.randn(*s, dtype=torch.bfloat16) * 0.1)(1, 1024) * 8.0), 'w_mod': _w((lambda *s: torch.randn(*s, dtype=torch.bfloat16) * 0.1)(1024, 6 * 1024) * 1024 ** (-0.5) * 8.0)}

def reference(A_x, A_wqkv, A_w_phi, A_wo, B_x, B_w1, B_w3, B_w2, c, w_mod):

    def _A_ref(x, wqkv, w_phi, wo):
        B, S, D = x.shape
        H = 32
        Dh = D // H
        qkv = (lambda x, *d: x.permute(*d))((x @ wqkv).reshape(B, S, 3, H, Dh), 2, 0, 3, 1, 4)
        q, k, v = (qkv[0], qkv[1], qkv[2])
        pq = torch.einsum('bhsd,hdf->bhsf', q, w_phi)
        pk = torch.einsum('bhsd,hdf->bhsf', k, w_phi)
        phi_q = torch.concatenate([_softmax(pq, axis=-1), _softmax(-pq, axis=-1)], axis=-1)
        phi_k = torch.concatenate([_softmax(pk, axis=-1), _softmax(-pk, axis=-1)], axis=-1)
        kv = torch.einsum('bhsf,bhsd->bhfd', phi_k, v)
        z = torch.einsum('bhsf->bhf', phi_k)
        num = torch.einsum('bhqf,bhfd->bhqd', phi_q, kv)
        den = torch.einsum('bhqf,bhf->bhq', phi_q, z)[..., None] + 1e-06
        o = num / den
        return (lambda x, *d: x.permute(*d))(o, 0, 2, 1, 3).reshape(B, S, D) @ wo

    def _B_ref(x, w1, w3, w2):
        gate = torch.einsum('bsd,df->bsf', x, w1)
        up = torch.einsum('bsd,df->bsf', x, w3)
        h = _silu(gate) * up
        out = torch.einsum('bsf,fd->bsd', h, w2)
        return out
    x = A_x
    mod = (_silu(c) @ w_mod).reshape(c.shape[0], 6, 1, -1)
    sa, ca, ga, sb, cb, gb = (mod[:, 0].clone(), mod[:, 1].clone(), mod[:, 2].clone(), mod[:, 3].clone(), mod[:, 4].clone(), mod[:, 5].clone())
    h = _layernorm(x, axis=-1) * (1 + ca) + sa
    x = x + ga * _A_ref(h, A_wqkv, A_w_phi, A_wo)
    h = _layernorm(x, axis=-1) * (1 + cb) + sb
    x = x + gb * _B_ref(h, B_w1, B_w3, B_w2)
    return x
```

```cpp
#include <hip/hip_runtime.h>
#include <stdint.h>
typedef unsigned short u16;
typedef __bf16 bf16x8 __attribute__((ext_vector_type(8)));
typedef __bf16 bf16x2 __attribute__((ext_vector_type(2)));
typedef short s16x4 __attribute__((ext_vector_type(4)));
typedef short s16x8 __attribute__((ext_vector_type(8)));
typedef float f32x2 __attribute__((ext_vector_type(2)));
typedef float f32x4 __attribute__((ext_vector_type(4)));
typedef unsigned u32x2 __attribute__((ext_vector_type(2)));
typedef unsigned u32x4 __attribute__((ext_vector_type(4)));
typedef __attribute__((address_space(1))) unsigned gu32;
typedef __attribute__((address_space(1))) unsigned long long gu64;
#define LAS __attribute__((address_space(3)))
#define DI __device__ __forceinline__
typedef __amdgpu_buffer_rsrc_t rsrc_t;

#define ASC1 1
#define WO_NS 5
#define WO_NW 9
#define PSLEEP 1
#define ROT 1
#define TOUCHQ touch
#define XLD __builtin_nontemporal_load
#define PF_CODE 1
#define QDUP 0
#define PREW_EARLY 4
#define YSPLIT 0
#define WO_PD 3
#define PREWAIT 1
#define GSLEEP 1
#define FDELAY 0
#define GDELAY 0
#define QLIN 0
#define SKH1 0
#define QDUP2 2
#define WEARLY 10
#define QUNROLL 3
#define WODUP 1
#define WS_SHIFT ((size_t)200 << 20)
#define SKHH 1
#define W2T 12
#define F2LZ 1
#define XPRIO 1
#define ESCHED 1
#define C3SPREAD 1
#define PF_QKV 1
#define PF_Q13 2
#define PF_P13 4
#define PF_T 13

#define TS_DECL
#define TS_PARAM
#define TS_ARG
#define TS(id)
#define TS_OUT()
constexpr int SD = 2048, DD = 1024, FD = 3584;

DI float bflo(unsigned v) { return __uint_as_float(v << 16); }
DI float bfhi(unsigned v) { return __uint_as_float(v & 0xffff0000u); }
DI unsigned cvtpk(float lo, float hi) { f32x2 v = {lo, hi}; bf16x2 b = __builtin_convertvector(v, bf16x2); return __builtin_bit_cast(unsigned, b); }
DI float rbf(float x) { return bflo(cvtpk(x, x)); }
DI unsigned rfl(unsigned x) { return __builtin_amdgcn_readfirstlane(x); }
DI rsrc_t mkrsrc(const void* p, unsigned bytes) { return __builtin_amdgcn_make_buffer_rsrc((void*)p, 0, bytes, 0x00020000); }
template <int SC1> DI void dma16(unsigned m0v, unsigned voff, rsrc_t srd, unsigned soff) {
  if (SC1 == 2) asm volatile("s_mov_b32 m0, %0\n\ts_nop 0\n\tbuffer_load_dwordx4 %1, %2, %3 offen nt lds" ::"s"(m0v), "v"(voff), "s"(srd), "s"(soff) : "memory"); else
  if (SC1) asm volatile("s_mov_b32 m0, %0\n\ts_nop 0\n\tbuffer_load_dwordx4 %1, %2, %3 offen sc1 lds" ::"s"(m0v), "v"(voff), "s"(srd), "s"(soff) : "memory");
  else asm volatile("s_mov_b32 m0, %0\n\ts_nop 0\n\tbuffer_load_dwordx4 %1, %2, %3 offen lds" ::"s"(m0v), "v"(voff), "s"(srd), "s"(soff) : "memory");
}
DI void touch(const void* p, unsigned ldsjunk) { asm volatile("s_mov_b32 m0, %1\n\ts_nop 0\n\tglobal_load_lds_dword %0, off" ::"v"(p), "s"(ldsjunk) : "memory"); }
DI void touch_nt(const void* p, unsigned ldsjunk) { asm volatile("s_mov_b32 m0, %1\n\ts_nop 0\n\tglobal_load_lds_dword %0, off nt" ::"v"(p), "s"(ldsjunk) : "memory"); }
DI void st16(rsrc_t r, unsigned off, u32x4 v) { __builtin_amdgcn_raw_buffer_store_b128(v, r, off, 0, 16); }
DI u32x4 ld16(rsrc_t r, unsigned off) { return __builtin_amdgcn_raw_buffer_load_b128(r, off, 0, 16); }
DI bf16x8 lds_rd128(unsigned addr) { return *(LAS bf16x8*)(uintptr_t)addr; }
DI s16x4 lds_tr(unsigned addr) { return __builtin_amdgcn_ds_read_tr16_b64_v4i16((LAS s16x4*)(uintptr_t)addr); }
DI bf16x8 cat8(s16x4 a, s16x4 b) { return __builtin_bit_cast(bf16x8, __builtin_shufflevector(a, b, 0, 1, 2, 3, 4, 5, 6, 7)); }
DI u32x4 pack8u(f32x4 a, f32x4 b) { return (u32x4){cvtpk(a[0], a[1]), cvtpk(a[2], a[3]), cvtpk(b[0], b[1]), cvtpk(b[2], b[3])}; }
DI bf16x8 pack8(f32x4 a, f32x4 b) { return __builtin_bit_cast(bf16x8, pack8u(a, b)); }
template <class T> DI T& LD(unsigned addr) { return *(T*)(LAS T*)(uintptr_t)addr; }
#define LDSV(T, addr) (*(LAS T*)(uintptr_t)(addr))
#define MFMA16(a, b, c) __builtin_amdgcn_mfma_f32_16x16x32_bf16((a), (b), (c), 0, 0, 0)
#define WAITV(n) asm volatile("s_waitcnt vmcnt(%0)" ::"n"(n) : "memory")
#define WAITL0() __builtin_amdgcn_s_waitcnt(0xC07F)
#define BAR() do { asm volatile("" ::: "memory"); __builtin_amdgcn_s_barrier(); asm volatile("" ::: "memory"); } while (0)
#define SCHED0() __builtin_amdgcn_sched_barrier(0)
#define RLX_AGENT __ATOMIC_RELAXED, __HIP_MEMORY_SCOPE_AGENT

DI float xg_max(float x) {
  auto s = __builtin_amdgcn_permlane16_swap(__float_as_uint(x), __float_as_uint(x), false, false); x = fmaxf(__uint_as_float(s[0]), __uint_as_float(s[1]));
  auto t = __builtin_amdgcn_permlane32_swap(__float_as_uint(x), __float_as_uint(x), false, false); return fmaxf(__uint_as_float(t[0]), __uint_as_float(t[1]));
}
DI float xg_sum(float x) {
  auto s = __builtin_amdgcn_permlane16_swap(__float_as_uint(x), __float_as_uint(x), false, false); x = __uint_as_float(s[0]) + __uint_as_float(s[1]);
  auto t = __builtin_amdgcn_permlane32_swap(__float_as_uint(x), __float_as_uint(x), false, false); return __uint_as_float(t[0]) + __uint_as_float(t[1]);
}
template <int CTRL> DI float dpp(float x) { return __builtin_bit_cast(float, __builtin_amdgcn_mov_dpp(__builtin_bit_cast(int, x), CTRL, 0xf, 0xf, true)); }
DI float row_sum(float x) { x += dpp<0xB1>(x); x += dpp<0x4E>(x); x += dpp<0x141>(x); return x + dpp<0x128>(x); }
DI float dot2(unsigned a, unsigned b, float c) { return __builtin_amdgcn_fdot2_f32_bf16(__builtin_bit_cast(bf16x2, a), __builtin_bit_cast(bf16x2, b), c, false); }
DI float sel4(int i, float a, float b, float c, float d) { const float t = (i & 1) ? b : a, u = (i & 1) ? d : c; return (i & 2) ? u : t; }
DI float ror8(float x) { return __builtin_bit_cast(float, __builtin_amdgcn_mov_dpp(__builtin_bit_cast(int, x), 0x128, 0xf, 0xf, true)); }
DI u32x4 widen(u32x2 X, u32x2 Y) {
  auto a = __builtin_amdgcn_permlane16_swap(X[0], Y[0], false, false); auto b = __builtin_amdgcn_permlane16_swap(X[1], Y[1], false, false);
  return (u32x4){a[0], b[0], a[1], b[1]};
}
constexpr int FSTR = 32;
typedef unsigned long long u64;
DI void wait_flags(const u64* f, u64 tok) {
  if (FDELAY) __builtin_amdgcn_s_sleep(FDELAY);
#pragma unroll 1
  for (unsigned spin = 0; spin < (1u << 20); ++spin) {
    if (__all(__hip_atomic_load((const gu64*)f, RLX_AGENT) == tok)) break;
    __builtin_amdgcn_s_sleep(PSLEEP);
  }
  asm volatile("" ::: "memory");
}
template <int NW> DI void publish(unsigned ldsctr, u64* flag, u64 tok, int lane) {
  WAITV(0);
  if (lane == 0) { if (__hip_atomic_fetch_add((LAS unsigned*)(uintptr_t)ldsctr, 1u, __ATOMIC_RELAXED, __HIP_MEMORY_SCOPE_WORKGROUP) == NW - 1) __hip_atomic_store((gu64*)flag, tok, RLX_AGENT); }
}
DI unsigned bid_qkv(unsigned mt, unsigned hp) { return ((mt >> 3) | ((hp >> 2) << 1)) + 8 * ((mt & 7) | ((hp & 3) << 3)); }
DI unsigned bid_wo(unsigned mt, unsigned ct) { return ((mt >> 2) | ((ct >> 3) << 2)) + 8 * ((mt & 3) | (((ct >> 1) & 3) << 2) | ((ct & 1) << 4)); }
DI unsigned bid_f1(unsigned mt, unsigned nt) { return ((mt >> 2) | ((nt >> 3) << 1)) + 8 * ((mt & 3) | ((nt & 7) << 2)); }

constexpr unsigned WS_MODT = 0  , WS_ST1 = 1 << 20, WS_STAT = 3 << 19  , WS_H1 = 2 << 20, WS_KVP = WS_H1 + (5 << 20), WS_OF = WS_KVP + (3 << 20),
                   WS_H2 = WS_OF + (4 << 20), WS_HH = WS_H2 + (4 << 20), WS_XB = WS_HH + (18 << 20), WS_END = WS_XB + (8 << 20), WS_CNT = WS_END;
constexpr int F_P0 = 0, F_H1 = 256, F_KV = 512, F_O = 768, F_S = 1024, F_Y = 1280, F_H = 1536, F_PAIR = 1792  , F_EPOCH = 2816;
constexpr int KVPB = 8192 + 512;

DI unsigned skipi(unsigned o) { const unsigned i = o >> 7, r = (i * 37u) >> 8, c = i - 7 * r; return ((r * 8 + c + (c >= 3 ? 1 : 0)) << 7) | (o & 127); }
template <int NP, int NS, int NW, int AOFF, int WOFF, bool NAT, int LZ, bool SK> struct Loader {
  static constexpr int WSZ = NP * 8192, PB = 2 * NP, PCS = 4 + PB;
  rsrc_t rA, rW; unsigned voffA[4], voffW[2], sa, sw, la, lw, ldb2, ps2;
  DI void init(const void* A, unsigned abytes, const void* W, unsigned wbytes, unsigned ldb, unsigned col0, unsigned pstride, unsigned mt, unsigned t0, unsigned lds0, unsigned w4, int lane) {
    rA = mkrsrc(A, abytes); rW = mkrsrc(W, wbytes); ldb2 = ldb * 2; ps2 = pstride * 2;
#pragma unroll
    for (int j = 0; j < 4; ++j) { const unsigned o = ((w4 & 1) * 4 + j) * 1024 + lane * 16; voffA[j] = SK ? skipi(o) : o; }
#pragma unroll
    for (int j = 0; j < 2; ++j) { const unsigned h = NAT ? (((lane >> 4) & 1) | (j << 1)) : ((lane >> 4) & 3); voffW[j] = (lane >> 3) * ldb2 + (((lane & 7) ^ (h << 1)) << 4); }
    sa = ((t0 * 2 + (w4 >> 1)) * 16 + mt) * (SK ? 10240 : 8192); sw = (t0 * 64 + w4 * 16) * ldb2 + col0 * 2;
    la = lds0 + AOFF + w4 * 4096; lw = lds0 + WOFF + w4 * 2048;
  }
  DI void issueW(unsigned off) {
#pragma unroll
    for (int j = 0; j < PB; ++j) dma16<0>(lw + off + (j >> 1) * 8192 + (j & 1) * 1024, voffW[j & 1], rW, sw + (j >> 1) * ps2 + (j & 1) * 8 * ldb2);
    sw += 64 * ldb2;
  }
  DI void issueA(unsigned off) {
#pragma unroll
    for (int j = 0; j < 4; ++j) dma16<ASC1>(la + off + j * 1024, voffA[j], rA, sa);
    sa += 32 * (SK ? 10240 : 8192);
  }
  DI void preW(int s0 = 0, int s1 = NW) {
#pragma unroll 1
    for (int s = s0; s < s1; ++s) issueW(s * WSZ);
  }
  template <int NT, int T> static constexpr int infl() {
    int n = 0;
    for (int u = T + 2; u < NS; ++u) n += 4;
    for (int i = 0; i < T - LZ; ++i) if (i + NS > T + 1) { if (i + NS < NT) n += 4; if (i + NW < NT) n += PB; }
    return n;
  }
  template <int NT, int T0, int T1> DI void steps(unsigned& ca, unsigned& cw) {
    if constexpr (T0 < T1) {
      WAITV((infl<NT, T0>())); BAR();
      if constexpr (T0 >= LZ && T0 - LZ + NW < NT) { issueW(cw); cw = cw == (NW - 1) * WSZ ? 0 : cw + WSZ; }
      if constexpr (T0 >= LZ && T0 - LZ + NS < NT) { issueA(ca); ca = ca == (NS - 1) * 16384 ? 0 : ca + 16384; }
      steps<NT, T0 + 1, T1>(ca, cw);
    }
  }
  template <int NT, int T0, int NTCH> DI void drain() { if constexpr (T0 < NT - 1) { WAITV((infl<NT, T0>() + NTCH)); BAR(); drain<NT, T0 + 1, NTCH>(); } }
  template <int NT, int NTCH, class F> DI void run(F tail) {
#pragma unroll
    for (int s = 0; s < NS; ++s) issueA(s * 16384);
    WAITV((NS - 1) * 4); BAR();
    unsigned ca = 0, cw = 0;
    static_assert(NS - 2 >= LZ, "ring too short");
    constexpr int L0 = NS - 2, L1 = NT - NW + LZ > L0 ? NT - NW + LZ : L0;
    steps<NT, 0, L0>(ca, cw);
#pragma unroll 1
    for (int t = L0; t < L1; ++t) { WAITV((infl<NT, L0>())); BAR(); issueW(cw); cw = cw == (NW - 1) * WSZ ? 0 : cw + WSZ; issueA(ca); ca = ca == (NS - 1) * 16384 ? 0 : ca + 16384; }
    steps<NT, L1, NT - NS + LZ>(ca, cw);
    tail();
    drain<NT, NT - NS + LZ, NTCH>();
  }
};
template <int NP, int NS, int NW, int AOFF, int WOFF, bool NAT, int LZ, int TID>
DI void g128_mma(const int nt, const unsigned lds0, const unsigned w4, const int lane, f32x4 (&acc)[4][2 * NP] TS_PARAM) {
  constexpr int NB = 2 * NP, WSZ = NP * 8192, NR = 4 + 2 * NB, NM = 4 * NB, HI = NAT ? 512 : 2048;
  const unsigned wm = w4 & 1, wn = w4 >> 1;
  const int g = lane >> 4, q = (lane & 15) >> 2, p = lane & 3;
  const unsigned aaddr = lds0 + AOFF + wm * 4096 + lane * 16, h = 2 * (g & 1) + (q >> 1);
  unsigned baddr[2];
#pragma unroll
  for (int j = 0; j < 2; ++j) baddr[j] = lds0 + WOFF + ((NAT ? 8 : 4) * g + q) * 128 + (((2 * wn + j) ^ h) << 5) + 8 * p;
#pragma unroll
  for (int m = 0; m < 4; ++m)
#pragma unroll
    for (int n = 0; n < NB; ++n) acc[m][n] = (f32x4){0.f, 0.f, 0.f, 0.f};
  bf16x8 a0[4], b0[NB], a1[4], b1[NB]; s16x4 tlo[NB];
  unsigned ra = aaddr, rb[2] = {baddr[0], baddr[1]};
  auto rd = [&](int i, bf16x8(&a)[4], bf16x8(&b)[NB], int s) {
    const int ib = i < 2 ? i : i - 1;
    if (i == 2) a[0] = lds_rd128(ra + s * 8192);
    else if (ib < 2 * NB) { const int n = ib >> 1; const unsigned ad = rb[n & 1] + (n >> 1) * 8192 + s * 4096; if (ib & 1) b[n] = cat8(tlo[n], lds_tr(ad + HI)); else tlo[n] = lds_tr(ad); }
    else a[i - 2 * NB] = lds_rd128(ra + s * 8192 + (i - 2 * NB) * 1024);
  };
  unsigned ca = 0, cw = 0;
  auto step = [&](bf16x8(&cA)[4], bf16x8(&cB)[NB], bf16x8(&na)[4], bf16x8(&nb)[NB], int s) {
#pragma unroll
    for (int i = 0; i < NM; ++i) {
      if (i == NM - 1 && !LZ) { WAITL0(); SCHED0(); }
      acc[i / NB][i % NB] = MFMA16(cB[i % NB], cA[i / NB], acc[i / NB][i % NB]);
      SCHED0();
      if (i < NR) { rd(i, na, nb, s); SCHED0(); }
      else if (s == 1 && i == NR) { ca = ca == (NS - 1) * 16384 ? 0 : ca + 16384; ra = aaddr + ca; asm volatile("" : "+v"(ra)); SCHED0(); }
      else if (s == 1 && i == NR + 1) { cw = cw == (NW - 1) * WSZ ? 0 : cw + WSZ; rb[0] = baddr[0] + cw; asm volatile("" : "+v"(rb[0])); SCHED0(); }
      else if (s == 1 && i == NR + 2) { rb[1] = baddr[1] + cw; asm volatile("" : "+v"(rb[1])); SCHED0(); }
    }
  };
  BAR();
  TS(TID);
  __builtin_amdgcn_s_setprio(1);
#pragma unroll
  for (int i = 0; i < NR; ++i) rd(i, a0, b0, 0);
  SCHED0();
  step(a0, b0, a1, b1, 1);
#pragma unroll 3
  for (int t = 0; t < nt - 1; ++t) {
    SCHED0(); BAR(); SCHED0();
    step(a1, b1, a0, b0, 0);
    step(a0, b0, a1, b1, 1);
  }
#pragma unroll
  for (int i = 0; i < NM; ++i) acc[i / NB][i % NB] = MFMA16(b1[i % NB], a1[i / NB], acc[i / NB][i % NB]);
  __builtin_amdgcn_s_setprio(0);
  TS(TID + 1);
}

DI float max3(float a, float b, float c) { float r; asm("v_max3_f32 %0, %1, %2, %3" : "=v"(r) : "v"(a), "v"(b), "v"(c)); return r; }
DI float max3n(float a, float b, float c) { float r; asm("v_max3_f32 %0, %1, %2, %3\n\ts_nop 1" : "=v"(r) : "v"(a), "v"(b), "v"(c)); return r; }
DI float min3(float a, float b, float c) { float r; asm("v_min3_f32 %0, %1, %2, %3" : "=v"(r) : "v"(a), "v"(b), "v"(c)); return r; }
DI void feature_map2(const bf16x8 (&xf)[2], const bf16x8 (&wf)[2], u32x4 (&pos)[2], u32x4 (&neg)[2]) {
  constexpr float L2E = 1.4426950408889634f;
  float p[2][8], mx[2], nm[2], m[2], mp[2], mn[2], ep[2][8], en[2][8], sp[2], sn[2];
  f32x4 t[2][2];
#pragma unroll
  for (int i = 0; i < 2; ++i)
#pragma unroll
    for (int fb = 0; fb < 2; ++fb) t[i][fb] = MFMA16(wf[fb], xf[i], ((f32x4){0.f, 0.f, 0.f, 0.f}));
  asm volatile("s_nop 7" : "+v"(t[0][0]), "+v"(t[0][1]), "+v"(t[1][0]), "+v"(t[1][1]));
#pragma unroll
  for (int i = 0; i < 2; ++i)
#pragma unroll
    for (int j = 0; j < 8; ++j) p[i][j] = t[i][j >> 2][j & 3];
#pragma unroll
  for (int i = 0; i < 2; ++i) { mx[i] = max3(max3(p[i][0], p[i][1], p[i][2]), max3(p[i][3], p[i][4], p[i][5]), max3(p[i][6], p[i][7], p[i][7]));
    nm[i] = -min3(min3(p[i][0], p[i][1], p[i][2]), min3(p[i][3], p[i][4], p[i][5]), min3(p[i][6], p[i][7], p[i][7])); }
  asm volatile("s_nop 1" : "+v"(mx[0]), "+v"(mx[1]), "+v"(nm[0]), "+v"(nm[1]));
#pragma unroll
  for (int i = 0; i < 2; ++i) { auto s1 = __builtin_amdgcn_permlane16_swap(__float_as_uint(mx[i]), __float_as_uint(nm[i]), false, false); m[i] = max3(__uint_as_float(s1[0]), __uint_as_float(s1[1]), __uint_as_float(s1[1])); }
  asm volatile("s_nop 1" : "+v"(m[0]), "+v"(m[1]));
#pragma unroll
  for (int i = 0; i < 2; ++i) { auto s2 = __builtin_amdgcn_permlane32_swap(__float_as_uint(m[i]), __float_as_uint(m[i]), false, false); m[i] = -L2E * max3(__uint_as_float(s2[0]), __uint_as_float(s2[1]), __uint_as_float(s2[1])); }
#pragma unroll
  for (int i = 0; i < 2; ++i) { auto s3 = __builtin_amdgcn_permlane16_swap(__float_as_uint(m[i]), __float_as_uint(m[i]), false, false); mp[i] = __uint_as_float(s3[0]); mn[i] = __uint_as_float(s3[1]); }
  SCHED0();
#pragma unroll
  for (int i = 0; i < 2; ++i)
#pragma unroll
    for (int j = 0; j < 8; j += 2) { const f32x2 pp = {p[i][j], p[i][j + 1]}, u = __builtin_elementwise_fma(pp, (f32x2){L2E, L2E}, (f32x2){mp[i], mp[i]}), v = __builtin_elementwise_fma(pp, (f32x2){-L2E, -L2E}, (f32x2){mn[i], mn[i]});
      ep[i][j] = __builtin_amdgcn_exp2f(u[0]); ep[i][j + 1] = __builtin_amdgcn_exp2f(u[1]); en[i][j] = __builtin_amdgcn_exp2f(v[0]); en[i][j + 1] = __builtin_amdgcn_exp2f(v[1]); }
#pragma unroll
  for (int i = 0; i < 2; ++i) {
    const f32x2 a = ((f32x2){ep[i][0], ep[i][1]} + (f32x2){ep[i][2], ep[i][3]}) + ((f32x2){ep[i][4], ep[i][5]} + (f32x2){ep[i][6], ep[i][7]});
    const f32x2 b = ((f32x2){en[i][0], en[i][1]} + (f32x2){en[i][2], en[i][3]}) + ((f32x2){en[i][4], en[i][5]} + (f32x2){en[i][6], en[i][7]});
    sp[i] = a[0] + a[1]; sn[i] = b[0] + b[1]; }
  SCHED0();
#pragma unroll
  for (int i = 0; i < 2; ++i) { auto t1 = __builtin_amdgcn_permlane16_swap(__float_as_uint(sp[i]), __float_as_uint(sn[i]), false, false); m[i] = __uint_as_float(t1[0]) + __uint_as_float(t1[1]); }
#pragma unroll
  for (int i = 0; i < 2; ++i) { auto t2 = __builtin_amdgcn_permlane32_swap(__float_as_uint(m[i]), __float_as_uint(m[i]), false, false); m[i] = __builtin_amdgcn_rcpf(__uint_as_float(t2[0]) + __uint_as_float(t2[1])); }
#pragma unroll
  for (int i = 0; i < 2; ++i) { auto t3 = __builtin_amdgcn_permlane16_swap(__float_as_uint(m[i]), __float_as_uint(m[i]), false, false); sp[i] = __uint_as_float(t3[0]); sn[i] = __uint_as_float(t3[1]); }
  SCHED0();
#pragma unroll
  for (int i = 0; i < 2; ++i)
#pragma unroll
    for (int j = 0; j < 4; ++j) { pos[i][j] = cvtpk(ep[i][2 * j] * sp[i], ep[i][2 * j + 1] * sp[i]); neg[i][j] = cvtpk(en[i][2 * j] * sn[i], en[i][2 * j + 1] * sn[i]); }
}
DI unsigned swiglu2(f32x2 x, f32x2 u) {
  const f32x2 t = x * -1.4426950408889634f, e = (f32x2){__builtin_amdgcn_exp2f(t[0]), __builtin_amdgcn_exp2f(t[1])} + 1.f, h = x * u * (f32x2){__builtin_amdgcn_rcpf(e[0]), __builtin_amdgcn_rcpf(e[1])};
  return cvtpk(h[0], h[1]);
}

constexpr int L_XS = 16384, L_META = 32768;
constexpr int L_WPH = 147456, L_MP3 = L_WPH + 4096, L_SV = L_MP3 + 8192;
constexpr int L_PH = 73728, L_VV = 32768, L_HO = L_PH + 32768, L_KVS = 131072, L_ZS = L_KVS + 8192;
constexpr int F_ABUF = 32768, F_BIMG = 14336, F_BBUF = 2 * F_BIMG, F_LDSB = 2 * F_ABUF;
constexpr int L_MODS = 161792, L_JUNK = L_MODS + 1280, L_CTR = L_JUNK + 256, LDS_TOTAL = L_CTR + 64;

struct Args { const u16 *x, *wqkv, *wphi, *wo, *w1, *w3, *w2, *c, *wmod; char* ws; u16* out; };

template <int N> DI void poll_granules(rsrc_t rWS, unsigned off, unsigned stride, unsigned off2, unsigned stride2, unsigned tag, u32x4 (&v)[N]) {
#pragma unroll 1
  for (unsigned spin = 0; spin < (1u << 20); ++spin) {
    bool ok = true;
#pragma unroll
    for (int i = 0; i < N; ++i) v[i] = ld16(rWS, i < 4 ? off + i * stride : off2 + (i - 4) * stride2);
#pragma unroll
    for (int i = 0; i < N; ++i) ok &= v[i][1] == tag && v[i][3] == tag;
    if (__all(ok)) break;
    asm volatile("s_sleep %0" ::"n"(GSLEEP) : "memory");
  }
}
DI void stats_put(const u32x4 (&y)[2], rsrc_t rWS, unsigned st, unsigned ct, unsigned tag, int lane) {
  float v[16];
#pragma unroll
  for (int j = 0; j < 8; ++j) { v[2 * j] = bflo(y[j >> 2][j & 3]); v[2 * j + 1] = bfhi(y[j >> 2][j & 3]); }
  const float mw = xg_sum((((v[0] + v[1]) + (v[2] + v[3])) + ((v[4] + v[5]) + (v[6] + v[7]))) + (((v[8] + v[9]) + (v[10] + v[11])) + ((v[12] + v[13]) + (v[14] + v[15])))) * (1.f / 64.f);
  float t0 = 0, t1 = 0;
#pragma unroll
  for (int j = 0; j < 8; ++j) { const float d0 = v[2 * j] - mw, d1 = v[2 * j + 1] - mw; t0 += d0 * d0; t1 += d1 * d1; }
  const float qq = xg_sum(t0 + t1);
  if (lane < 16) st16(rWS, st + ct * 256 + lane * 16, (u32x4){__float_as_uint(mw), tag, __float_as_uint(qq), tag});
}
template <int N> DI f32x2 stats_fin(const u32x4 (&e)[N]) {
  const float mean = xg_sum((__uint_as_float(e[0][0]) + __uint_as_float(e[1][0])) + (__uint_as_float(e[2][0]) + __uint_as_float(e[3][0]))) * (1.f / 16.f);
  float q = 0;
#pragma unroll
  for (int i = 0; i < 4; ++i) { const float d = __uint_as_float(e[i][0]) - mean; q += __uint_as_float(e[i][2]) + 64.f * d * d; }
  return (f32x2){mean, rsqrtf(xg_sum(q) * (1.f / 1024.f) + 1e-5f)};
}
DI f32x4 lo4(u32x4 v) { return (f32x4){bflo(v[0]), bfhi(v[0]), bflo(v[1]), bfhi(v[1])}; }
DI f32x4 hi4(u32x4 v) { return (f32x4){bflo(v[2]), bfhi(v[2]), bflo(v[3]), bfhi(v[3])}; }
DI void ln_store(const u32x4 (&y)[2], f32x2 st, unsigned ldssh, unsigned ldssc, rsrc_t rWS, unsigned off, unsigned hstep) {
#pragma unroll
  for (int h = 0; h < 2; ++h)
    st16(rWS, off + h * hstep, pack8u((lo4(y[h]) - st[0]) * st[1] * LDSV(f32x4, ldssc + h * 128) + LDSV(f32x4, ldssh + h * 128), (hi4(y[h]) - st[0]) * st[1] * LDSV(f32x4, ldssc + h * 128 + 64) + LDSV(f32x4, ldssh + h * 128 + 64)));
}
template <bool TAGGED> DI void mod_panel(unsigned panel, const bf16x8 (&sf)[2], rsrc_t rWS, unsigned off, unsigned tag, int lane) {
  const int fr = lane & 15, g = lane >> 4, q = fr >> 2, p = lane & 3; const unsigned h = 2 * (g & 1) + (q >> 1);
  f32x4 r[4];
#pragma unroll
  for (int b = 0; b < 4; ++b) { const unsigned ad = panel + (4 * g + q) * 128 + ((b ^ h) << 5) + 8 * p;
    r[b] = MFMA16(cat8(lds_tr(ad), lds_tr(ad + 2048)), sf[0], ((f32x4){0.f, 0.f, 0.f, 0.f})); r[b] = MFMA16(cat8(lds_tr(ad + 4096), lds_tr(ad + 6144)), sf[1], r[b]); }
  const f32x4 v = {sel4(p, r[0][0], r[1][0], r[2][0], r[3][0]), sel4(p, r[0][1], r[1][1], r[2][1], r[3][1]), sel4(p, r[0][2], r[1][2], r[2][2], r[3][2]), sel4(p, r[0][3], r[1][3], r[2][3], r[3][3])};
  if (TAGGED) { if (q < 2) st16(rWS, off + (p * 8 + 2 * g + q) * 16, (u32x4){__float_as_uint(q ? v[2] : v[0]), tag, __float_as_uint(q ? v[3] : v[1]), tag}); }
  else if (q == 0) st16(rWS, off + (p * 16 + 4 * g) * 4, __builtin_bit_cast(u32x4, v));
}

template <int ROLE>
DI void ffn1_wave(const Args& a, unsigned mt, unsigned nt, const unsigned (&voff)[7], const rsrc_t rS, const rsrc_t rZ, unsigned sbase, unsigned ldsd, unsigned lds0, unsigned wr, int lane TS_PARAM) {
  constexpr unsigned wc = ROLE, SSTEP = ROLE == 0 ? 64 * FD * 2 : 2 * 128 * 1024;
  auto issue = [&](unsigned ldsoff, rsrc_t r, unsigned so) {
    if (ROLE == 0) {
#pragma unroll
      for (int j = 0; j < 7; ++j) dma16<0>(ldsd + ldsoff + j * 1024, voff[j], r, so);
    } else {
#pragma unroll
      for (int i = 0; i < 8; ++i) dma16<1>(ldsd + ldsoff + i * 1024, voff[0], r, so + i * 1024);
    }
  };
  if (ROLE == 1) issue(0, rS, sbase);
  BAR();
  issue(ROLE == 0 ? F_BBUF : F_ABUF, rS, sbase + SSTEP);
  const int g = lane >> 4, q = (lane & 15) >> 2, p = lane & 3;
  const unsigned aaddr = lds0 + wr * 4096 + lane * 16;
  const unsigned baddr = lds0 + F_LDSB + (4 * g + q) * 224 + wc * 128 + 8 * p;
  const unsigned maddr = lds0 + F_LDSB + (4 * g + q) * 224 + 96 + wc * 16 + 8 * (p & 1) + (p >> 1) * F_BIMG;
  f32x4 acc[4][7];
#pragma unroll
  for (int m = 0; m < 4; ++m)
#pragma unroll
    for (int n = 0; n < 7; ++n) acc[m][n] = (f32x4){0.f, 0.f, 0.f, 0.f};
  bf16x8 a0[4], b0[7], a1[4], b1[7]; s16x4 tlo[7];
  auto rd = [&](int i, bf16x8(&av)[4], bf16x8(&b)[7], int s, unsigned aoff, unsigned boff) {
    if (i < 14) { const int n = i >> 1; const unsigned ad = (n < 6 ? baddr + (n % 3) * 32 + (n / 3) * F_BIMG : maddr) + boff + s * 32 * 224;
      if (i & 1) b[n] = cat8(tlo[n], lds_tr(ad + 16 * 224)); else tlo[n] = lds_tr(ad); }
    else av[i - 14] = lds_rd128(aaddr + aoff + s * 16384 + (i - 14) * 1024);
  };
  constexpr int HOLD = ROLE == 1 ? YSPLIT : 0;
  auto step = [&](bf16x8(&ca)[4], bf16x8(&cb)[7], bf16x8(&na)[4], bf16x8(&nb)[7], int s, unsigned aoff, unsigned boff) {
    const int last = s == 1 ? 28 - HOLD : 28;
#pragma unroll
    for (int i = 0; i < 28; ++i) if (i < last) {
      if (i == last - 1) { WAITL0(); SCHED0(); }
      acc[i / 7][i % 7] = MFMA16(cb[i % 7], ca[i / 7], acc[i / 7][i % 7]);
      SCHED0();
      if (i < 18) { rd(i, na, nb, s, aoff, boff); SCHED0(); }
    }
  };
  auto held = [&]() {
#pragma unroll
    for (int i = 28 - HOLD; i < 28; ++i) acc[i / 7][i % 7] = MFMA16(b0[i % 7], a0[i / 7], acc[i / 7][i % 7]);
    SCHED0();
  };
  WAITV(ROLE == 0 ? 7 : 8); BAR();
  TS(16);
  if (ROLE == 0) __builtin_amdgcn_s_setprio(1);
#pragma unroll
  for (int i = 0; i < 18; ++i) rd(i, a0, b0, 0, 0, 0);
  unsigned aoff = 0, boff = 0, so = sbase + (ROLE == 0 ? 2 : 1) * SSTEP;
  SCHED0();
  step(a0, b0, a1, b1, 1, aoff, boff);
  if (ROLE == 0) { issue(2 * F_BBUF, rS, sbase + 2 * SSTEP); SCHED0(); }
  if (ROLE == 1) WAITV(0);
#pragma unroll 1
  for (int t = 0; t < 15; ++t) {
    if (HOLD) __builtin_amdgcn_s_setprio(2);
    if (ROLE == 0 && t == 14) WAITV(0);
    WAITV(ROLE == 0 ? 7 : 1); SCHED0(); BAR(); SCHED0();
    held();
    if (HOLD) { __builtin_amdgcn_s_setprio(0); SCHED0(); }
    const unsigned poff = ROLE == 0 ? boff : aoff; aoff ^= F_ABUF; boff = boff == 2 * F_BBUF ? 0 : boff + F_BBUF; so += SSTEP;
    if (ROLE == 1 && t < 14) {
      issue(poff, rS, so);
      const unsigned tt = t < 9 ? t : 9, l14 = wr * 14 + (lane < 14 ? lane : 13);
      const char* tp = tt < 6 ? (const char*)((tt & 1) ? a.w3 : a.w1) + (size_t)((256 + (tt >> 1) * 256 + blockIdx.x) * 56 + l14) * 128 : (const char*)a.w2 + (size_t)(((tt - 6) * 256 + blockIdx.x) * 56 + l14) * 128;
      if (W2T && t >= W2T) tp = (const char*)a.w2 + (size_t)((((t - W2T) & 1) * 32 + (blockIdx.x >> 3)) * 56 + l14) * 2048 + ((blockIdx.x >> 2) & 1) * 1024 + ((3 - ((uintptr_t)a.w2 >> 7)) & 7) * 128;
      touch(tp, lds0 + L_JUNK);
      SCHED0();
    }
    step(a1, b1, a0, b0, 0, aoff, boff);
    step(a0, b0, a1, b1, 1, aoff, boff);
    if (ROLE == 0 && t < 13) { issue(poff, rS, so); SCHED0(); }
  }
  held();
  TS(3); SCHED0();
#pragma unroll
  for (int i = 0; i < 28; ++i) acc[i / 7][i % 7] = MFMA16(b1[i % 7], a1[i / 7], acc[i / 7][i % 7]);
  if (!XPRIO) { if (ROLE == 0) __builtin_amdgcn_s_setprio(0); else __builtin_amdgcn_s_setprio(1); }
  if (ESCHED) SCHED0();
  TS(17);
  if (ESCHED) SCHED0();
  const rsrc_t rH = mkrsrc(a.ws + WS_HH, 18 << 20);
  constexpr unsigned SLOT = SKHH ? 10240 : 8192;
  const unsigned hb = (mt * 2 + (wr >> 1)) * SLOT, ho = (wr & 1) * 4096 + (lane & 15) * 16;
  auto hst = [&](unsigned B, unsigned par, unsigned o, u32x4 v) { o += ho + par * 512; __builtin_amdgcn_raw_buffer_store_b128(v, rH, SKHH ? skipi(o) : o, hb + (B >> 1) * 16 * SLOT, 16); asm volatile("s_nop 1" ::"v"(v) : "memory"); };
  const unsigned par0 = nt & 1;
  u32x2 mx[2];
#pragma unroll
  for (int mp = 0; mp < 2; ++mp) {
#pragma unroll
    for (int n = 0; n < 3; ++n) {
      const unsigned B = 7 * nt + 4 * wc + n;
      u32x2 w[2];
#pragma unroll
      for (int i = 0; i < 2; ++i) { const f32x4 G = acc[2 * mp + i][n], U = acc[2 * mp + i][3 + n]; w[i] = (u32x2){swiglu2((f32x2){G[0], G[1]}, (f32x2){U[0], U[1]}), swiglu2((f32x2){G[2], G[3]}, (f32x2){U[2], U[3]})}; }
      hst(B, par0 ^ (n & 1), (2 * mp + (g & 1)) * 1024 + (g >> 1) * 256, widen(w[0], w[1]));
    }
    float mg[4], mu[4];
#pragma unroll
    for (int e = 0; e < 4; ++e) { auto r = __builtin_amdgcn_permlane32_swap(__float_as_uint(acc[2 * mp][6][e]), __float_as_uint(acc[2 * mp + 1][6][e]), false, false); mg[e] = __uint_as_float(r[0]); mu[e] = __uint_as_float(r[1]); }
    mx[mp] = (u32x2){swiglu2((f32x2){mg[0], mg[1]}, (f32x2){mu[0], mu[1]}), swiglu2((f32x2){mg[2], mg[3]}, (f32x2){mu[2], mu[3]})};
  }
  { const unsigned B = 7 * nt + 3; hst(B, par0 ^ 1, (2 * (g & 1) + (g >> 1)) * 1024 + wc * 256, widen(mx[0], mx[1])); }
  SCHED0(); TS(9);
  __builtin_amdgcn_s_setprio(0);
}

__global__ __launch_bounds__(512, 2) void k_main(const Args a) {
  extern __shared__ __attribute__((aligned(16))) char smem[];
  const int tid = threadIdx.x, lane = tid & 63;
  const unsigned wid = rfl(tid >> 6), lds0 = (unsigned)(uintptr_t)(LAS char*)smem, w4 = wid & 3;
  const bool isL = wid >= 4;
  const unsigned bid = blockIdx.x, xcd = bid & 7, loc = bid >> 3;
  const int fr = lane & 15, g = lane >> 4, q = fr >> 2, p = lane & 3;
  const unsigned wm = w4 & 1, wn = w4 >> 1;
  TS_DECL;
  TS(0);
  if (wid == 6) {
    u64 pc; unsigned len;
    asm volatile("s_getpc_b64 %0\n.Lcode_ref:\n\ts_mov_b32 %1, .Lcode_end-.Lcode_ref" : "=s"(pc), "=s"(len));
    const unsigned o = ((lane < 13 ? lane : 12) * 32 + loc) * 128;
    touch((const char*)pc + (o < len ? o : 0), lds0 + L_JUNK);
  }
  asm volatile("" ::"s"(a.x), "s"(a.ws), "s"(a.out));
  u64* const flg = (u64*)(a.ws + WS_CNT);
  const rsrc_t rWS = mkrsrc(a.ws, WS_END);
  const unsigned mtq = 8 * (xcd & 1) + (loc & 7), hp = 4 * (xcd >> 1) + (loc >> 3);
  const unsigned mto = 4 * (xcd & 3) + (loc & 3), cto = 8 * (xcd >> 2) + 2 * ((loc >> 2) & 3) + (loc >> 4);
  const unsigned mtf = 4 * (xcd & 1) + (loc & 3), ntf = 8 * (xcd >> 1) + (loc >> 2);
  const unsigned ks2 = cto & 1;
  const unsigned mbl = 2 * w4 + (wid >> 2), mbo = mto * 8 + mbl;
  const rsrc_t rM = mkrsrc(a.wmod, DD * 6 * DD * 2);
  const unsigned voffM = (lane >> 3) * (6 * DD * 2) + (((lane & 7) ^ (((lane >> 4) & 3) << 1)) << 4), ctm = cto ^ (w4 + 1)  , soM = mto * 64 * (6 * DD * 2) + ctm * 128;
  { const void* mp = lane < 32 ? (const void*)(a.c + mto * 64 + lane * 2) : (const void*)((const unsigned*)(flg + F_EPOCH * FSTR) + (lane & 1));
    asm volatile("s_mov_b32 m0, %1\n\ts_nop 0\n\tglobal_load_lds_dword %0, off sc1" ::"v"(mp), "s"(lds0 + L_META + wid * 256) : "memory"); }
#pragma unroll
  for (int i = 0; i < 2; ++i) dma16<2>(lds0 + L_XS + wid * 2048 + i * 1024, (lane >> 3) * (DD * 2) + (((lane & 7) ^ (lane >> 3)) << 4), mkrsrc(a.x, SD * DD * 2), (mbo * 16 + i * 8) * (DD * 2) + cto * 128);
  if (wid == 4 || wid == 5) {
#pragma unroll
    for (int i = 0; i < 8; ++i) dma16<2>(lds0 + w4 * 8192 + i * 1024, voffM, rM, soM + i * 8 * (6 * DD * 2) + w4 * DD * 2);
  }
  if (tid < 16) LDSV(unsigned, lds0 + L_CTR + tid * 4) = 0u;
  WAITL0(); BAR();
  if (wid == 6) {
#pragma unroll
    for (int i = 0; i < 4; ++i) dma16<0>(lds0 + L_WPH + i * 1024, lane * 16, mkrsrc(a.wphi, 65536), hp * 4096 + i * 1024);
    WAITV(4);
  } else if (wid == 7) {
    const unsigned c3 = (3 - ((uintptr_t)a.wqkv >> 7)) & 7;
#pragma unroll
    for (int i = 0; i < 3; ++i) {
      const unsigned f = ((i * 2 + (xcd & 1)) * 32 + loc) * 64 + lane, row = (f * 43691u) >> 19, r = f - row * 12, cl = (xcd >> 1) * 4 + (r & 3);
      touch((const char*)a.wqkv + (size_t)row * 6144 + (r >> 2) * 2048 + ((C3SPREAD && (cl & 7) == c3) ? cl ^ 1 : cl) * 128, lds0 + L_JUNK);
    }
    { const unsigned e = (lane < 24 ? lane : 23) * 256 + bid; touch((const char*)a.wqkv + (size_t)e * 1024 + c3 * 128, lds0 + L_JUNK); }
    WAITV(4);
  } else if (isL) { WAITV(8); } else { WAITV(0); }
  u64 tok;
  { const u32x2 e = LDSV(u32x2, lds0 + L_META + wid * 256 + 128); u64 z = (((u64)rfl(e[1]) << 32) | rfl(e[0])) + 0x9E3779B97F4A7C15ull; z = (z ^ (z >> 30)) * 0xBF58476D1CE4E5B9ull; tok = z ^ (z >> 27); }
  const unsigned tag = (unsigned)(tok >> 16);
  u32x4 y[2];
#pragma unroll
  for (int h = 0; h < 2; ++h) { const unsigned xa = lds0 + L_XS + wid * 2048 + fr * 128 + (g & 1) * 8;
    const u32x2 lo = LDSV(u32x2, xa + (((4 * h + (g >> 1)) ^ (fr & 7)) << 4)), hi = LDSV(u32x2, xa + (((4 * h + 2 + (g >> 1)) ^ (fr & 7)) << 4)); y[h] = (u32x4){lo[0], lo[1], hi[0], hi[1]}; }
  stats_put(y, rWS, WS_ST1 + mbo * 4096, cto, tag, lane);
  bf16x8 sf[2];
  if (isL) {
    const float v = bflo(LDSV(u16, lds0 + L_META + wid * 256 + lane * 2)); LDSV(u16, lds0 + L_SV + 512 + w4 * 128 + lane * 2) = (u16)cvtpk(v * __builtin_amdgcn_rcpf(1.f + __expf(-v)), 0.f);
#pragma unroll
    for (int s = 0; s < 2; ++s) { const u32x2 lo = LDSV(u32x2, lds0 + L_SV + 512 + w4 * 128 + s * 64 + g * 8), hi = LDSV(u32x2, lds0 + L_SV + 512 + w4 * 128 + s * 64 + 32 + g * 8);
      sf[s] = __builtin_bit_cast(bf16x8, ((u32x4){lo[0], lo[1], hi[0], hi[1]})); }
    if (w4 < 2) { WAITV(1); mod_panel<true>(lds0 + w4 * 8192, sf, rWS, WS_MODT + ((w4 * 16 + ctm) * 16 + mto) * 512, tag, lane); }
  }
  if (PREWAIT) WAITV(0);
  TS(1);
  {
    u32x4 e[6];
    poll_granules<6>(rWS, WS_ST1 + mbo * 4096 + g * 1024 + fr * 16, 256, WS_MODT + (((wid >> 2) * 16 + cto) * 16 + (lane >> 3)) * 512 + (w4 * 8 + (lane & 7)) * 16, 8 * 512, tag, e);
    float m0 = __uint_as_float(e[4][0]) + __uint_as_float(e[5][0]), m1 = __uint_as_float(e[4][2]) + __uint_as_float(e[5][2]);
    m0 = rbf(xg_sum(m0 + ror8(m0))); m1 = rbf(xg_sum(m1 + ror8(m1)));
    if (isL) { m0 = rbf(1.f + m0); m1 = rbf(1.f + m1); }
    if (lane < 8) LDSV(f32x2, lds0 + L_SV + wid * 64 + lane * 8) = (f32x2){m0, m1};
    const f32x2 st = stats_fin(e);
    WAITL0(); BAR();
    TS(26);
    ln_store(y, st, lds0 + L_SV + g * 16, lds0 + L_SV + 256 + g * 16, rWS, SKH1 ? WS_H1 + (cto * 32 + mto) * 10240 + skipi(mbl * 1024 + lane * 16) : WS_H1 + (cto * 2 * 128 + mbo) * 1024 + lane * 16, SKH1 ? 163840 : 131072);
    asm volatile("" : "+v"(y[0]), "+v"(y[1]));
    TS(27);
    publish<8>(lds0 + L_CTR, flg + (F_H1 + bid) * FSTR, tok, lane);
    TS(2);
  }
  bf16x8 qf[2], kf2[2], wf[2];
#pragma unroll
  for (int fb = 0; fb < 2; ++fb) { const unsigned ad = lds0 + L_WPH + wn * 2048 + (4 * g + q) * 64 + fb * 32 + 8 * p; wf[fb] = cat8(lds_tr(ad), lds_tr(ad + 1024)); }
  if (isL) {
    Loader<3, 3, 4, 0, 49152, false, 0, SKH1> ld; ld.init(a.ws + WS_H1, 5 << 20, a.wqkv, DD * 3 * DD * 2, 3 * DD, hp * 64, DD, mtq, 0, lds0, w4, lane);
    ld.preW();
    BAR();
    TS(3);
    ld.run<16, 9>([&]() {
#pragma unroll
      for (int i = 0; i < 8; ++i) dma16<2>(lds0 + (w4 < 3 ? 49152 + w4 * 8192 : L_MP3) + i * 1024, voffM, rM, soM + i * 8 * (6 * DD * 2) + (2 + w4) * DD * 2);
      { const unsigned e = loc * 256 + (xcd & 3) * 64 + w4 * 16 + (lane & 15); touch((const char*)a.wo + (size_t)(e >> 3) * 2048 + (xcd >> 2) * 1024 + (e & 7) * 128, lds0 + L_JUNK); }
    });
    BAR();
    touch((const char*)((w4 & 1) ? a.w3 : a.w1) + (size_t)(bid * 56 + (w4 >> 1) * 28 + (lane < 28 ? lane : 27)) * 128, lds0 + L_JUNK);
    { const unsigned e = loc * 256 + w4 * 64 + lane; touch((const char*)a.wo + (size_t)(e >> 3) * 2048 + (xcd >> 2) * 1024 + (e & 7) * 128, lds0 + L_JUNK); }
#pragma unroll
    for (int i = 0; i < 2; ++i) { kf2[i] = lds_rd128(lds0 + L_HO + (w4 * 4 + i) * 1024 + lane * 16); qf[i] = lds_rd128(lds0 + L_HO + (w4 * 4 + 2 + i) * 1024 + lane * 16); }
  } else {
    { const unsigned c3 = (3 - ((uintptr_t)a.wqkv >> 7)) & 7;
      if (wid != 0 && (c3 >> 2) == ((xcd >> 1) & 1)) touch((const char*)a.wqkv + (size_t)(256 + loc * 24 + (lane < 24 ? lane : 23)) * 6144 + (wid - 1) * 2048 + (xcd >> 2) * 1024 + c3 * 128, lds0 + L_JUNK); }
    if (wid == 0) wait_flags(flg + (F_H1 + bid_wo(mtq, fr)) * FSTR, tok);
    BAR();
    f32x4 acc[4][6];
    g128_mma<3, 3, 4, 0, 49152, false, 0, 4>(16, lds0, w4, lane, acc TS_ARG);
#pragma unroll
    for (int i = 0; i < 2; ++i) {
      LDSV(u32x4, lds0 + L_HO + (w4 * 4 + i) * 1024 + lane * 16) = pack8u(acc[2 + i][2], acc[2 + i][3]);
      LDSV(u32x4, lds0 + L_HO + (w4 * 4 + 2 + i) * 1024 + lane * 16) = pack8u(acc[2 + i][0], acc[2 + i][1]);
    }
    WAITL0(); BAR();
    { const unsigned va = lds0 + L_VV + wn * 8192 + (wm * 64 + fr) * 64 + ((g ^ (((fr >> 1) & 1) | ((fr >> 3) << 1))) << 3), sw = (fr >> 2) & 1;
#pragma unroll
      for (int m = 0; m < 4; ++m)
#pragma unroll
        for (int db = 0; db < 2; ++db) { const f32x4 v = acc[m][4 + db]; LDSV(u32x2, va + m * 1024 + ((db ^ sw) << 5)) = (u32x2){cvtpk(v[0], v[1]), cvtpk(v[2], v[3])}; } }
#pragma unroll
    for (int i = 0; i < 2; ++i) { kf2[i] = pack8(acc[i][2], acc[i][3]); qf[i] = pack8(acc[i][0], acc[i][1]); }
  }
  {
    TS(28);
    const unsigned mloc = wm * 4 + (isL ? 2 : 0);
    {
      u32x4 pos[2], neg[2];
      feature_map2(kf2, wf, pos, neg);
      const unsigned pa = lds0 + L_PH + wn * 16384 + (mloc * 16 + fr) * 128 + ((g ^ ((fr & 1) | ((fr >> 3) << 1))) << 3), sw = (fr >> 1) & 3;
#pragma unroll
      for (int i = 0; i < 2; ++i) {
        LDSV(u32x2, pa + i * 2048 + ((0 ^ sw) << 5)) = (u32x2){pos[i][0], pos[i][1]}; LDSV(u32x2, pa + i * 2048 + ((1 ^ sw) << 5)) = (u32x2){pos[i][2], pos[i][3]};
        LDSV(u32x2, pa + i * 2048 + ((2 ^ sw) << 5)) = (u32x2){neg[i][0], neg[i][1]}; LDSV(u32x2, pa + i * 2048 + ((3 ^ sw) << 5)) = (u32x2){neg[i][2], neg[i][3]};
      }
    }
    WAITL0(); BAR();
    TS(29);
    {
      const unsigned hh = wid >> 2, rr = 4 * g + q;
      const unsigned pa = lds0 + L_PH + hh * 16384 + rr * 128 + ((w4 ^ ((rr >> 1) & 3)) << 5) + ((p ^ ((rr & 1) | ((rr >> 3) << 1))) << 3);
      const unsigned va = lds0 + L_VV + hh * 8192 + rr * 64 + ((p ^ (((rr >> 1) & 1) | ((rr >> 3) << 1))) << 3), vsw = (rr >> 2) & 1;
      f32x4 kv[3] = {{0.f, 0.f, 0.f, 0.f}, {0.f, 0.f, 0.f, 0.f}, {0.f, 0.f, 0.f, 0.f}};
      const bf16x8 ones = __builtin_bit_cast(bf16x8, ((u32x4){0x3f803f80u, 0x3f803f80u, 0x3f803f80u, 0x3f803f80u}));
#pragma unroll
      for (int ks = 0; ks < 4; ++ks) {
        const bf16x8 pf = cat8(lds_tr(pa + ks * 4096), lds_tr(pa + ks * 4096 + 2048));
#pragma unroll
        for (int db = 0; db < 2; ++db) { const unsigned ad = va + ks * 2048 + ((db ^ vsw) << 5); kv[db] = MFMA16(pf, cat8(lds_tr(ad), lds_tr(ad + 1024)), kv[db]); }
        kv[2] = MFMA16(pf, ones, kv[2]);
      }
      const unsigned dst = WS_KVP + (hp * 16 + mtq) * KVPB;
      st16(rWS, dst + tid * 16, pack8u(kv[0], kv[1]));
      if (fr == 0) st16(rWS, dst + 8192 + wid * 64 + g * 16, __builtin_bit_cast(u32x4, kv[2]));
    }
    TS(30);
    publish<8>(lds0 + L_CTR + 4, flg + (F_KV + bid) * FSTR, tok, lane);
    TS(6);
    {
      const rsrc_t rW = mkrsrc(a.wo, DD * DD * 2);
      const unsigned voffW = (lane >> 3) * (DD * 2) + (((lane & 7) ^ (((lane >> 4) & 3) << 1)) << 4);
#pragma unroll
      for (int i = 0; i < 7; ++i) { const unsigned pc = (i < 4 ? i : i + 9) * 8 + wid; dma16<0>(lds0 + pc * 1024, voffW, rW, pc * 8 * (DD * 2) + cto * 128); }
    }
    if (isL) mod_panel<true>(lds0 + (w4 < 3 ? 49152 + w4 * 8192 : L_MP3), sf, rWS, WS_MODT + (((2 + w4) * 16 + ctm) * 16 + mto) * 512, tag, lane);
    u32x4 php[2], phn[2];
    feature_map2(qf, wf, php, phn);
    if (wid == 0) wait_flags(flg + (F_KV + bid_qkv(fr, hp)) * FSTR, tok);
    BAR();
    TS(7);
    {
      u32x4 v[16];
#pragma unroll
      for (int i = 0; i < 16; ++i) v[i] = ld16(rWS, WS_KVP + (hp * 16 + ((i + ROT * 2 * mtq) & 15)) * KVPB + tid * 16);
      float zv[4];
#pragma unroll
      for (int i = 0; i < 4; ++i) zv[i] = __uint_as_float(__builtin_amdgcn_raw_buffer_load_b32(rWS, WS_KVP + (hp * 16 + 4 * g + i) * KVPB + 8192 + (wid * 16 + fr) * 4, 0, 16));
      float s[8] = {0, 0, 0, 0, 0, 0, 0, 0};
      unsigned elo = 0x00003f80u, ehi = 0x3f800000u; asm volatile("" : "+v"(elo), "+v"(ehi));
#pragma unroll
      for (int i = 0; i < 16; ++i)
#pragma unroll
        for (int j = 0; j < 4; ++j) { s[2 * j] = dot2(v[i][j], elo, s[2 * j]); s[2 * j + 1] = dot2(v[i][j], ehi, s[2 * j + 1]); }
      LDSV(u32x4, lds0 + L_KVS + tid * 16) = (u32x4){cvtpk(s[0], s[1]), cvtpk(s[2], s[3]), cvtpk(s[4], s[5]), cvtpk(s[6], s[7])};
      const float z = xg_sum((zv[0] + zv[1]) + (zv[2] + zv[3]));
      if (g == 0) LDSV(u16, lds0 + L_ZS + (wid * 16 + fr) * 2) = (u16)cvtpk(z, z);
    }
    WAITL0(); BAR();
    TS(31);
    bf16x8 kf[2][3];
#pragma unroll
    for (int ks = 0; ks < 2; ++ks) {
      const u32x4 u0 = LDSV(u32x4, lds0 + L_KVS + (wn * 4 + 2 * ks) * 1024 + lane * 16), u1 = LDSV(u32x4, lds0 + L_KVS + (wn * 4 + 2 * ks + 1) * 1024 + lane * 16);
      kf[ks][0] = __builtin_bit_cast(bf16x8, ((u32x4){u0[0], u0[1], u1[0], u1[1]})); kf[ks][1] = __builtin_bit_cast(bf16x8, ((u32x4){u0[2], u0[3], u1[2], u1[3]}));
      const u32x2 za = LDSV(u32x2, lds0 + L_ZS + wn * 128 + ks * 64 + g * 8), zb = LDSV(u32x2, lds0 + L_ZS + wn * 128 + ks * 64 + 32 + g * 8);
      kf[ks][2] = __builtin_bit_cast(bf16x8, ((u32x4){za[0], za[1], zb[0], zb[1]}));
    }
#pragma unroll
    for (int i = 0; i < 2; ++i) {
      f32x4 o[3];
#pragma unroll
      for (int db = 0; db < 3; ++db) { o[db] = MFMA16(kf[0][db], __builtin_bit_cast(bf16x8, php[i]), ((f32x4){0.f, 0.f, 0.f, 0.f})); o[db] = MFMA16(kf[1][db], __builtin_bit_cast(bf16x8, phn[i]), o[db]); }
      const float inv = __builtin_amdgcn_rcpf(o[2][0] + 1e-6f);
      st16(rWS, WS_OF + ((2 * hp + wn) * 128 + mtq * 8 + mloc + i) * 1024 + lane * 16, pack8u(o[0] * inv, o[1] * inv));
    }
    TS(15);
    publish<8>(lds0 + L_CTR + 8, flg + (F_O + bid) * FSTR, tok, lane);
    TS(8);
  }
  if (isL) {
    const rsrc_t rW = mkrsrc(a.wo, DD * DD * 2);
    const unsigned voffW = (lane >> 3) * (DD * 2) + (((lane & 7) ^ (((lane >> 4) & 3) << 1)) << 4);
#pragma unroll 2
    for (int i = 8; i < 26; ++i) dma16<0>(lds0 + (i * 4 + w4) * 1024, voffW, rW, (i * 4 + w4) * 8 * (DD * 2) + cto * 128);
    WAITV(0);
  } else {
    { u32x4 e[8];
      const unsigned mo = WS_MODT + (((2 + w4) * 16 + cto) * 16) * 512 + lane * 16;
      poll_granules<8>(rWS, mo, 1024, mo + 4096, 1024, tag, e);
      float m0 = 0, m1 = 0;
#pragma unroll
      for (int i = 0; i < 8; ++i) { m0 += __uint_as_float(e[i][0]); m1 += __uint_as_float(e[i][2]); }
      auto t0 = __builtin_amdgcn_permlane32_swap(__float_as_uint(m0), __float_as_uint(m1), false, false);
      float mv = rbf(__uint_as_float(t0[0]) + __uint_as_float(t0[1])); if (w4 == 2) mv = rbf(1.f + mv);
      LDSV(float, lds0 + L_MODS + w4 * 256 + (lane & 31) * 8 + (lane >> 5) * 4) = mv; }
    if (wid == 0) wait_flags(flg + (F_O + bid_qkv(mto, fr)) * FSTR, tok);
    if (bid == 0 && tid == 0) __hip_atomic_store((gu64*)(flg + F_EPOCH * FSTR), tok, RLX_AGENT);
    WAITL0();
  }
  BAR();
  TS(10);
  {
    constexpr int PD = WO_PD;
    u32x4 af[16][2]; bf16x8 bf[2][4];
    const unsigned kh = wid >> 2, ao = WS_OF + (kh * 16 * 128 + mbo) * 1024 + lane * 16, h = 2 * (g & 1) + (q >> 1);
    unsigned ba[4];
#pragma unroll
    for (int n = 0; n < 4; ++n) { ba[n] = lds0 + kh * 65536 + (4 * g + q) * 128 + ((n ^ h) << 5) + 8 * p; asm volatile("" : "+v"(ba[n])); }
#pragma unroll
    for (int ks = 0; ks < PD; ++ks) { af[ks][0] = __builtin_amdgcn_raw_buffer_load_b128(rWS, ao, ks * 131072, 16); af[ks][1] = __builtin_amdgcn_raw_buffer_load_b128(rWS, ao ^ 1024, ks * 131072, 16); }
#pragma unroll
    for (int n = 0; n < 4; ++n) bf[0][n] = cat8(lds_tr(ba[n]), lds_tr(ba[n] + 2048));
    SCHED0();
    f32x4 acc[4], oth[4];
#pragma unroll
    for (int n = 0; n < 4; ++n) { acc[n] = (f32x4){0.f, 0.f, 0.f, 0.f}; oth[n] = acc[n]; }
#pragma unroll
    for (int ks = 0; ks < 16; ++ks) {
      if (ks + PD < 16) { af[ks + PD][0] = __builtin_amdgcn_raw_buffer_load_b128(rWS, ao, (ks + PD) * 131072, 16); af[ks + PD][1] = __builtin_amdgcn_raw_buffer_load_b128(rWS, ao ^ 1024, (ks + PD) * 131072, 16); }
      if (ks + 1 < 16) {
#pragma unroll
        for (int n = 0; n < 4; ++n) { const unsigned ad = ba[n] + (ks + 1) * 4096; bf[(ks + 1) & 1][n] = cat8(lds_tr(ad), lds_tr(ad + 2048)); }
      }
      if (ks == 15) { WAITL0(); BAR(); }
      SCHED0();
#pragma unroll
      for (int n = 0; n < 4; ++n) oth[n] = MFMA16(bf[ks & 1][n], __builtin_bit_cast(bf16x8, af[ks][1]), oth[n]);
#pragma unroll
      for (int n = 0; n < 4; ++n) acc[n] = MFMA16(bf[ks & 1][n], __builtin_bit_cast(bf16x8, af[ks][0]), acc[n]);
      SCHED0();
    }
#pragma unroll
    for (int n = 0; n < 4; ++n) LDSV(f32x4, lds0 + (wid * 4 + n) * 1024 + lane * 16) = oth[n];
    WAITL0(); BAR();
#pragma unroll
    for (int n = 0; n < 4; ++n) acc[n] += LDSV(f32x4, lds0 + ((wid ^ 4) * 4 + n) * 1024 + lane * 16);
    TS(11);
#pragma unroll
    for (int hh = 0; hh < 2; ++hh) y[hh] = pack8u(lo4(y[hh]) + LDSV(f32x4, lds0 + L_MODS + hh * 128 + g * 16) * acc[2 * hh], hi4(y[hh]) + LDSV(f32x4, lds0 + L_MODS + hh * 128 + 64 + g * 16) * acc[2 * hh + 1]);
    stats_put(y, rWS, WS_STAT + mbo * 4096, cto, tag, lane);
    if (PREWAIT) WAITV(0);
    TS(12);
    u32x4 e[4];
    poll_granules<4>(rWS, WS_STAT + mbo * 4096 + g * 1024 + fr * 16, 256, 0, 0, tag, e);
    const f32x2 st = stats_fin(e);
    TS(25);
    ln_store(y, st, lds0 + L_MODS + 256 + g * 16, lds0 + L_MODS + 512 + g * 16, rWS, WS_H2 + (cto * 2 * 128 + mbo) * 1024 + lane * 16, 131072);
    asm volatile("" : "+v"(y[0]), "+v"(y[1]));
    publish<8>(lds0 + L_CTR + 16, flg + (F_Y + bid) * FSTR, tok, lane);
    TS(13);
  }
  if (isL) {
    unsigned voff[7];
#pragma unroll
    for (int j = 0; j < 7; ++j) { const unsigned i = j * 64 + lane, k = (i * 4682u) >> 16, c = i - k * 14; voff[j] = k * (FD * 2) + c * 16; }
    const u16* src = w4 < 2 ? a.w1 : a.w3;
    const rsrc_t rS = mkrsrc(src, DD * FD * 2), rZ = mkrsrc(src, 0);
    const unsigned sbase = ntf * 224 + (w4 & 1) * 32 * (FD * 2), ldsd = lds0 + F_LDSB + w4 * 7 * 1024;
#pragma unroll
    for (int j = 0; j < 7; ++j) dma16<0>(ldsd + j * 1024, voff[j], rS, sbase);
    BAR();
    ffn1_wave<0>(a, mtf, ntf, voff, rS, rZ, sbase, ldsd, lds0, w4, lane TS_ARG);
  } else {
    if (wid == 0) wait_flags(flg + (F_Y + bid_wo(2 * mtf + (g & 1), fr)) * FSTR, tok);
    BAR();
    TS(14);
    unsigned voff[7] = {(unsigned)lane * 16, 0, 0, 0, 0, 0, 0};
    const rsrc_t rS = mkrsrc(a.ws + WS_H2, SD * DD * 2), rZ = mkrsrc(a.ws + WS_H2, 0);
    ffn1_wave<1>(a, mtf, ntf, voff, rS, rZ, ((w4 >> 1) * 128 + mtf * 16 + (w4 & 1) * 8) * 1024, lds0 + w4 * 8192, lds0, w4, lane TS_ARG);
  }
  publish<8>(lds0 + L_CTR + 20, flg + (F_H + bid) * FSTR, tok, lane);
  BAR();
  TS(18);
  if (isL) {
    Loader<2, 4, 5, 0, 65536, true, F2LZ, SKHH> ld; ld.init(a.ws + WS_HH, 18 << 20, a.w2, FD * DD * 2, DD, cto * 64, ks2 ? -64 : 64, mto, ks2 * 28, lds0, w4, lane);
    ld.preW();
    BAR();
    TS(19);
    ld.run<28, 0>([]() {});
  } else {
    if (wid == 0) wait_flags(flg + (F_H + bid_f1(mto >> 1, ks2 * 16 + fr)) * FSTR, tok);
    BAR();
    f32x4 acc[4][4];
    g128_mma<2, 4, 5, 0, 65536, true, F2LZ, 20>(28, lds0, w4, lane, acc TS_ARG);
#pragma unroll
    for (int i = 0; i < 8; ++i) { const f32x4 v = acc[i >> 1][2 + (i & 1)];
      st16(rWS, WS_XB + (bid * 32 + (wm * 4 + (i >> 1)) * 4 + wn * 2 + (i & 1)) * 1024 + lane * 16, (u32x4){cvtpk(v[0], v[1]), tag, cvtpk(v[2], v[3]), tag}); }
#pragma unroll
    for (int i = 0; i < 8; ++i) LDSV(f32x4, lds0 + ((wm * 4 + (i >> 1)) * 4 + wn * 2 + (i & 1)) * 1024 + lane * 16) = acc[i >> 1][i & 1];
    if (PREWAIT) WAITV(0);
    WAITL0();
  }
  BAR();
  TS(22);
  {
    f32x4 gt[4], s[4];
#pragma unroll
    for (int n = 0; n < 4; ++n) { gt[n] = LDSV(f32x4, lds0 + L_MODS + 768 + n * 64 + g * 16); s[n] = ((n & 1) ? hi4(y[n >> 1]) : lo4(y[n >> 1])) + gt[n] * LDSV(f32x4, lds0 + (mbl * 4 + n) * 1024 + lane * 16); }
    u32x4 o[4];
    poll_granules<4>(rWS, WS_XB + ((bid ^ 128) * 32 + mbl * 4) * 1024 + lane * 16, 1024, 0, 0, tag, o);
    TS(23);
#pragma unroll
    for (int n = 0; n < 4; ++n) s[n] += gt[n] * (f32x4){bflo(o[n][0]), bfhi(o[n][0]), bflo(o[n][2]), bfhi(o[n][2])};
#pragma unroll
    for (int h = 0; h < 2; ++h)
      *(u32x4*)(a.out + (size_t)(mbo * 16 + fr) * DD + cto * 64 + h * 32 + (g & 1) * 16 + (g >> 1) * 8) =
          widen((u32x2){cvtpk(s[2 * h][0], s[2 * h][1]), cvtpk(s[2 * h][2], s[2 * h][3])}, (u32x2){cvtpk(s[2 * h + 1][0], s[2 * h + 1][1]), cvtpk(s[2 * h + 1][2], s[2 * h + 1][3])});
    TS(24); TS_OUT();
    asm volatile(".Lcode_end:");
  }
}

extern "C" void kernel_launch(void* const* d_in, const int* in_sizes, int n_in, void* d_out, int out_size, void* d_ws, size_t ws_size, hipStream_t stream) {
  Args a;
  a.x = (const u16*)d_in[0]; a.wqkv = (const u16*)d_in[1]; a.wphi = (const u16*)d_in[2]; a.wo = (const u16*)d_in[3]; a.w1 = (const u16*)d_in[5]; a.w3 = (const u16*)d_in[6]; a.w2 = (const u16*)d_in[7];
  a.c = (const u16*)d_in[8]; a.wmod = (const u16*)d_in[9]; a.ws = (char*)d_ws + WS_SHIFT; a.out = (u16*)d_out;
  (void)hipFuncSetAttribute((const void*)k_main, hipFuncAttributeMaxDynamicSharedMemorySize, LDS_TOTAL);
  k_main<<<256, 512, LDS_TOTAL, stream>>>(a);
}
```

```cpp
#include <hip/hip_runtime.h>
#include <stdint.h>
typedef unsigned short u16;
typedef __bf16 bf16x8 __attribute__((ext_vector_type(8)));
typedef __bf16 bf16x2 __attribute__((ext_vector_type(2)));
typedef short s16x4 __attribute__((ext_vector_type(4)));
typedef float f32x2 __attribute__((ext_vector_type(2)));
typedef float f32x4 __attribute__((ext_vector_type(4)));
typedef unsigned u32x2 __attribute__((ext_vector_type(2)));
typedef unsigned u32x4 __attribute__((ext_vector_type(4)));
typedef __attribute__((address_space(1))) unsigned long long gu64;
#define LAS __attribute__((address_space(3)))
#define DI __device__ __forceinline__
typedef __amdgpu_buffer_rsrc_t rsrc_t;

constexpr int SD = 2048, DD = 1024, FD = 3584;

DI float bflo(unsigned v) { return __uint_as_float(v << 16); }
DI float bfhi(unsigned v) { return __uint_as_float(v & 0xffff0000u); }
DI unsigned cvtpk(float lo, float hi) { f32x2 v = {lo, hi}; bf16x2 b = __builtin_convertvector(v, bf16x2); return __builtin_bit_cast(unsigned, b); }
DI float rbf(float x) { return bflo(cvtpk(x, x)); }
DI unsigned rfl(unsigned x) { return __builtin_amdgcn_readfirstlane(x); }
DI rsrc_t mkrsrc(const void* p, unsigned bytes) { return __builtin_amdgcn_make_buffer_rsrc((void*)p, 0, bytes, 0x00020000); }
template <int SC1> DI void dma16(unsigned m0v, unsigned voff, rsrc_t srd, unsigned soff) {
  if (SC1 == 2) asm volatile("s_mov_b32 m0, %0\n\ts_nop 0\n\tbuffer_load_dwordx4 %1, %2, %3 offen nt lds" ::"s"(m0v), "v"(voff), "s"(srd), "s"(soff) : "memory"); else
  if (SC1) asm volatile("s_mov_b32 m0, %0\n\ts_nop 0\n\tbuffer_load_dwordx4 %1, %2, %3 offen sc1 lds" ::"s"(m0v), "v"(voff), "s"(srd), "s"(soff) : "memory");
  else asm volatile("s_mov_b32 m0, %0\n\ts_nop 0\n\tbuffer_load_dwordx4 %1, %2, %3 offen lds" ::"s"(m0v), "v"(voff), "s"(srd), "s"(soff) : "memory");
}
DI void touch(const void* p, unsigned ldsjunk) { asm volatile("s_mov_b32 m0, %1\n\ts_nop 0\n\tglobal_load_lds_dword %0, off" ::"v"(p), "s"(ldsjunk) : "memory"); }
DI void st16(rsrc_t r, unsigned off, u32x4 v) { __builtin_amdgcn_raw_buffer_store_b128(v, r, off, 0, 16); }
DI u32x4 ld16(rsrc_t r, unsigned off) { return __builtin_amdgcn_raw_buffer_load_b128(r, off, 0, 16); }
DI bf16x8 lds_rd128(unsigned addr) { return *(LAS bf16x8*)(uintptr_t)addr; }
DI s16x4 lds_tr(unsigned addr) { return __builtin_amdgcn_ds_read_tr16_b64_v4i16((LAS s16x4*)(uintptr_t)addr); }
DI bf16x8 cat8(s16x4 a, s16x4 b) { return __builtin_bit_cast(bf16x8, __builtin_shufflevector(a, b, 0, 1, 2, 3, 4, 5, 6, 7)); }
DI u32x4 pack8u(f32x4 a, f32x4 b) { return (u32x4){cvtpk(a[0], a[1]), cvtpk(a[2], a[3]), cvtpk(b[0], b[1]), cvtpk(b[2], b[3])}; }
DI bf16x8 pack8(f32x4 a, f32x4 b) { return __builtin_bit_cast(bf16x8, pack8u(a, b)); }
#define LDSV(T, addr) (*(LAS T*)(uintptr_t)(addr))
#define MFMA16(a, b, c) __builtin_amdgcn_mfma_f32_16x16x32_bf16((a), (b), (c), 0, 0, 0)
#define WAITV(n) asm volatile("s_waitcnt vmcnt(%0)" ::"n"(n) : "memory")
#define WAITL0() __builtin_amdgcn_s_waitcnt(0xC07F)
#define BAR() do { asm volatile("" ::: "memory"); __builtin_amdgcn_s_barrier(); asm volatile("" ::: "memory"); } while (0)
#define SCHED0() __builtin_amdgcn_sched_barrier(0)
#define RLX_AGENT __ATOMIC_RELAXED, __HIP_MEMORY_SCOPE_AGENT

DI float xg_sum(float x) {
  auto s = __builtin_amdgcn_permlane16_swap(__float_as_uint(x), __float_as_uint(x), false, false); x = __uint_as_float(s[0]) + __uint_as_float(s[1]);
  auto t = __builtin_amdgcn_permlane32_swap(__float_as_uint(x), __float_as_uint(x), false, false); return __uint_as_float(t[0]) + __uint_as_float(t[1]);
}
DI float dot2(unsigned a, unsigned b, float c) { return __builtin_amdgcn_fdot2_f32_bf16(__builtin_bit_cast(bf16x2, a), __builtin_bit_cast(bf16x2, b), c, false); }
DI float sel4(int i, float a, float b, float c, float d) { const float t = (i & 1) ? b : a, u = (i & 1) ? d : c; return (i & 2) ? u : t; }
DI float ror8(float x) { return __builtin_bit_cast(float, __builtin_amdgcn_mov_dpp(__builtin_bit_cast(int, x), 0x128, 0xf, 0xf, true)); }
DI u32x4 widen(u32x2 X, u32x2 Y) {
  auto a = __builtin_amdgcn_permlane16_swap(X[0], Y[0], false, false); auto b = __builtin_amdgcn_permlane16_swap(X[1], Y[1], false, false);
  return (u32x4){a[0], b[0], a[1], b[1]};
}
constexpr int FSTR = 32;
typedef unsigned long long u64;
DI void wait_flags(const u64* f, u64 tok) {
#pragma unroll 1
  for (unsigned spin = 0; spin < (1u << 20); ++spin) {
    if (__all(__hip_atomic_load((const gu64*)f, RLX_AGENT) == tok)) break;
    __builtin_amdgcn_s_sleep(1);
  }
  asm volatile("" ::: "memory");
}
template <int NW> DI void publish(unsigned ldsctr, u64* flag, u64 tok, int lane) {
  WAITV(0);
  if (lane == 0) { if (__hip_atomic_fetch_add((LAS unsigned*)(uintptr_t)ldsctr, 1u, __ATOMIC_RELAXED, __HIP_MEMORY_SCOPE_WORKGROUP) == NW - 1) __hip_atomic_store((gu64*)flag, tok, RLX_AGENT); }
}
DI unsigned bid_qkv(unsigned mt, unsigned hp) { return ((mt >> 3) | ((hp >> 2) << 1)) + 8 * ((mt & 7) | ((hp & 3) << 3)); }
DI unsigned bid_wo(unsigned mt, unsigned ct) { return ((mt >> 2) | ((ct & 1) << 2)) + 8 * ((mt & 3) | ((ct >> 1) << 2)); }
DI unsigned bid_f1(unsigned mt, unsigned nt) { return ((mt >> 2) | ((nt >> 3) << 1)) + 8 * ((mt & 3) | ((nt & 7) << 2)); }


constexpr unsigned WS_MODT = 0  , WS_ST1 = 1 << 20, WS_STAT = 3 << 19  , WS_H1 = 2 << 20, WS_KVP = WS_H1 + (4 << 20), WS_OF = WS_KVP + (4 << 20),
                   WS_H2 = WS_OF + (4 << 20), WS_HH = WS_H2 + (4 << 20), WS_XB = WS_HH + (18 << 20), WS_END = WS_XB + (4 << 20), WS_CNT = WS_END;
constexpr int F_H1 = 0, F_KV = 256, F_O = 512, F_Y = 768, F_H = 1024, F_EPOCH = 1280;
constexpr int KVPB = 8192 + 512;

DI unsigned skipi(unsigned o) { const unsigned i = o >> 7, r = (i * 37u) >> 8, c = i - 7 * r; return ((r * 8 + c + (c >= 3 ? 1 : 0)) << 7) | (o & 127); }
template <int NP, int NS, int NW, int AOFF, int WOFF, bool NAT, int LZ, bool SK> struct Loader {
  static constexpr int WSZ = NP * 8192, PB = 2 * NP, PA = 4, ASZ = 16384;
  rsrc_t rA, rW; unsigned voffA[4], voffW[2], sa, sw, la, lw, ldb2, ps2;
  DI void init(const void* A, unsigned abytes, const void* W, unsigned wbytes, unsigned ldb, unsigned col0, unsigned pstride, unsigned mt, unsigned t0, unsigned lds0, unsigned w4, int lane) {
    rA = mkrsrc(A, abytes); rW = mkrsrc(W, wbytes); ldb2 = ldb * 2; ps2 = pstride * 2;
#pragma unroll
    for (int j = 0; j < 4; ++j) { const unsigned o = ((w4 & 1) * 4 + j) * 1024 + lane * 16; voffA[j] = SK ? skipi(o) : o; }
#pragma unroll
    for (int j = 0; j < 2; ++j) { const unsigned h = NAT ? (((lane >> 4) & 1) | (j << 1)) : ((lane >> 4) & 3); voffW[j] = (lane >> 3) * ldb2 + (((lane & 7) ^ (h << 1)) << 4); }
    sa = ((t0 * 2 + (w4 >> 1)) * 16 + mt) * (SK ? 10240 : 8192); sw = (t0 * 64 + w4 * 16) * ldb2 + col0 * 2;
    la = lds0 + AOFF + w4 * 4096; lw = lds0 + WOFF + w4 * 2048;
  }
  DI void issueW(unsigned off) {
#pragma unroll
    for (int j = 0; j < PB; ++j) dma16<0>(lw + off + (j >> 1) * 8192 + (j & 1) * 1024, voffW[j & 1], rW, sw + (j >> 1) * ps2 + (j & 1) * 8 * ldb2);
    sw += 64 * ldb2;
  }
  DI void issueA(unsigned off) {
#pragma unroll
    for (int j = 0; j < PA; ++j) dma16<1>(la + off + j * 1024, voffA[j], rA, sa);
    sa += 32 * (SK ? 10240 : 8192);
  }
  template <int NPRE> DI void preW() {
#pragma unroll 1
    for (int s = 0; s < NPRE; ++s) issueW(s * WSZ);
  }
  template <int NT, int NPRE, int T> static constexpr int infl() {
    int n = 0;
    for (int u = T + 2; u < NS; ++u) n += PA;
    if (T + 1 < NS) for (int u = NPRE; u < NW; ++u) if (u > T + 1) n += PB;
    for (int i = 0; i < T - LZ; ++i) if (i + NS > T + 1) { if (i + NS < NT) n += PA; if (i + NW < NT) n += PB; }
    return n;
  }
  template <int NT, int NPRE, int T0, int T1> DI void steps(unsigned& ca, unsigned& cw) {
    if constexpr (T0 < T1) {
      WAITV((infl<NT, NPRE, T0>())); BAR();
      if constexpr (T0 >= LZ && T0 - LZ + NW < NT) { issueW(cw); cw = cw == (NW - 1) * WSZ ? 0 : cw + WSZ; }
      if constexpr (T0 >= LZ && T0 - LZ + NS < NT) { issueA(ca); ca = ca == (NS - 1) * ASZ ? 0 : ca + ASZ; }
      steps<NT, NPRE, T0 + 1, T1>(ca, cw);
    }
  }
  template <int NT, int NPRE, int T0, int NTCH> DI void drain() { if constexpr (T0 < NT - 1) { WAITV((infl<NT, NPRE, T0>() + NTCH)); BAR(); drain<NT, NPRE, T0 + 1, NTCH>(); } }
  template <int NT, int NPRE, int NTCH, class F> DI void run(F tail) {
    constexpr int N0 = NPRE - 1 < 2 ? NPRE - 1 : 2;
#pragma unroll
    for (int s = 0; s <= N0; ++s) issueA(s * ASZ);
    WAITV(N0 * PA); BAR();
#pragma unroll
    for (int s = N0 + 1; s < NW; ++s) { if (s >= NPRE) issueW(s * WSZ); if (s < NS) issueA(s * ASZ); }
    unsigned ca = 0, cw = 0;
    static_assert(NS - 2 >= LZ, "ring too short");
    constexpr int L0 = NS - 1, L1 = NT - NW + LZ > L0 ? NT - NW + LZ : L0;
    steps<NT, NPRE, 0, L0>(ca, cw);
#pragma unroll 1
    for (int t = L0; t < L1; ++t) { WAITV((infl<NT, NPRE, L0>())); BAR(); issueW(cw); cw = cw == (NW - 1) * WSZ ? 0 : cw + WSZ; issueA(ca); ca = ca == (NS - 1) * ASZ ? 0 : ca + ASZ; }
    steps<NT, NPRE, L1, NT - NS + LZ>(ca, cw);
    tail();
    drain<NT, NPRE, NT - NS + LZ, NTCH>();
  }
};
template <int NP, int NS, int NW, int AOFF, int WOFF, bool NAT, int LZ>
DI void g128_mma(const int nt, const unsigned lds0, const unsigned w4, const int lane, f32x4 (&acc)[4][2 * NP]) {
  constexpr int NB = 2 * NP, WSZ = NP * 8192, MB = 4, ASZ = 16384, NR = MB + 2 * NB, NM = MB * NB, HI = NAT ? 512 : 2048;
  const unsigned wm = w4 & 1, wn = w4 >> 1;
  const int g = lane >> 4, q = (lane & 15) >> 2, p = lane & 3;
  const unsigned aaddr = lds0 + AOFF + wm * 4096 + lane * 16, h = 2 * (g & 1) + (q >> 1);
  unsigned baddr[2];
#pragma unroll
  for (int j = 0; j < 2; ++j) baddr[j] = lds0 + WOFF + ((NAT ? 8 : 4) * g + q) * 128 + (((2 * wn + j) ^ h) << 5) + 8 * p;
#pragma unroll
  for (int m = 0; m < MB; ++m)
#pragma unroll
    for (int n = 0; n < NB; ++n) acc[m][n] = (f32x4){0.f, 0.f, 0.f, 0.f};
  bf16x8 a0[MB], b0[NB], a1[MB], b1[NB]; s16x4 tlo[NB];
  unsigned ra = aaddr, rb[2] = {baddr[0], baddr[1]};
  auto rd = [&](int i, bf16x8(&a)[MB], bf16x8(&b)[NB], int s) {
    const int ib = i < 2 ? i : i - 1;
    if (i == 2) a[0] = lds_rd128(ra + s * 8192);
    else if (ib < 2 * NB) { const int n = ib >> 1; const unsigned ad = rb[n & 1] + (n >> 1) * 8192 + s * 4096; if (ib & 1) b[n] = cat8(tlo[n], lds_tr(ad + HI)); else tlo[n] = lds_tr(ad); }
    else { const int m = i - 2 * NB; a[m] = lds_rd128(ra + s * 8192 + m * 1024); }
  };
  unsigned ca = 0, cw = 0;
  auto step = [&](bf16x8(&cA)[MB], bf16x8(&cB)[NB], bf16x8(&na)[MB], bf16x8(&nb)[NB], int s) {
#pragma unroll
    for (int i = 0; i < NM; ++i) {
      if (i == NM - 1 && !LZ) { WAITL0(); SCHED0(); }
      acc[i / NB][i % NB] = MFMA16(cB[i % NB], cA[i / NB], acc[i / NB][i % NB]);
      SCHED0();
      if (i < NR) { rd(i, na, nb, s); SCHED0(); }
      else if (s == 1 && i == NR) { ca = ca == (NS - 1) * ASZ ? 0 : ca + ASZ; ra = aaddr + ca; asm volatile("" : "+v"(ra)); SCHED0(); }
      else if (s == 1 && i == NR + 1) { cw = cw == (NW - 1) * WSZ ? 0 : cw + WSZ; rb[0] = baddr[0] + cw; asm volatile("" : "+v"(rb[0])); SCHED0(); }
      else if (s == 1 && i == NR + 2) { rb[1] = baddr[1] + cw; asm volatile("" : "+v"(rb[1])); SCHED0(); }
    }
  };
  BAR();
  __builtin_amdgcn_s_setprio(1);
#pragma unroll
  for (int i = 0; i < NR; ++i) rd(i, a0, b0, 0);
  SCHED0();
  step(a0, b0, a1, b1, 1);
#pragma unroll 3
  for (int t = 0; t < nt - 1; ++t) {
    SCHED0(); BAR(); SCHED0();
    step(a1, b1, a0, b0, 0);
    step(a0, b0, a1, b1, 1);
  }
#pragma unroll
  for (int i = NM - 1; i >= 0; --i) acc[i / NB][i % NB] = MFMA16(b1[i % NB], a1[i / NB], acc[i / NB][i % NB]);
  __builtin_amdgcn_s_setprio(0);
}

DI float max3(float a, float b, float c) { float r; asm("v_max3_f32 %0, %1, %2, %3" : "=v"(r) : "v"(a), "v"(b), "v"(c)); return r; }
DI float min3(float a, float b, float c) { float r; asm("v_min3_f32 %0, %1, %2, %3" : "=v"(r) : "v"(a), "v"(b), "v"(c)); return r; }
DI void feature_map2(const bf16x8 (&xf)[2], const bf16x8 (&wf)[2], u32x4 (&pos)[2], u32x4 (&neg)[2]) {
  constexpr float L2E = 1.4426950408889634f;
  float p[2][8], mx[2], nm[2], m[2], mp[2], mn[2], ep[2][8], en[2][8], sp[2], sn[2];
  f32x4 t[2][2];
#pragma unroll
  for (int i = 0; i < 2; ++i)
#pragma unroll
    for (int fb = 0; fb < 2; ++fb) t[i][fb] = MFMA16(wf[fb], xf[i], ((f32x4){0.f, 0.f, 0.f, 0.f}));
  asm volatile("s_nop 7" : "+v"(t[0][0]), "+v"(t[0][1]), "+v"(t[1][0]), "+v"(t[1][1]));
#pragma unroll
  for (int i = 0; i < 2; ++i)
#pragma unroll
    for (int j = 0; j < 8; ++j) p[i][j] = t[i][j >> 2][j & 3];
#pragma unroll
  for (int i = 0; i < 2; ++i) { mx[i] = max3(max3(p[i][0], p[i][1], p[i][2]), max3(p[i][3], p[i][4], p[i][5]), max3(p[i][6], p[i][7], p[i][7]));
    nm[i] = -min3(min3(p[i][0], p[i][1], p[i][2]), min3(p[i][3], p[i][4], p[i][5]), min3(p[i][6], p[i][7], p[i][7])); }
  asm volatile("s_nop 1" : "+v"(mx[0]), "+v"(mx[1]), "+v"(nm[0]), "+v"(nm[1]));
#pragma unroll
  for (int i = 0; i < 2; ++i) { auto s1 = __builtin_amdgcn_permlane16_swap(__float_as_uint(mx[i]), __float_as_uint(nm[i]), false, false); m[i] = max3(__uint_as_float(s1[0]), __uint_as_float(s1[1]), __uint_as_float(s1[1])); }
  asm volatile("s_nop 1" : "+v"(m[0]), "+v"(m[1]));
#pragma unroll
  for (int i = 0; i < 2; ++i) { auto s2 = __builtin_amdgcn_permlane32_swap(__float_as_uint(m[i]), __float_as_uint(m[i]), false, false); m[i] = -L2E * max3(__uint_as_float(s2[0]), __uint_as_float(s2[1]), __uint_as_float(s2[1])); }
#pragma unroll
  for (int i = 0; i < 2; ++i) { auto s3 = __builtin_amdgcn_permlane16_swap(__float_as_uint(m[i]), __float_as_uint(m[i]), false, false); mp[i] = __uint_as_float(s3[0]); mn[i] = __uint_as_float(s3[1]); }
  SCHED0();
#pragma unroll
  for (int i = 0; i < 2; ++i)
#pragma unroll
    for (int j = 0; j < 8; j += 2) { const f32x2 pp = {p[i][j], p[i][j + 1]}, u = __builtin_elementwise_fma(pp, (f32x2){L2E, L2E}, (f32x2){mp[i], mp[i]}), v = __builtin_elementwise_fma(pp, (f32x2){-L2E, -L2E}, (f32x2){mn[i], mn[i]});
      ep[i][j] = __builtin_amdgcn_exp2f(u[0]); ep[i][j + 1] = __builtin_amdgcn_exp2f(u[1]); en[i][j] = __builtin_amdgcn_exp2f(v[0]); en[i][j + 1] = __builtin_amdgcn_exp2f(v[1]); }
#pragma unroll
  for (int i = 0; i < 2; ++i) {
    const f32x2 a = ((f32x2){ep[i][0], ep[i][1]} + (f32x2){ep[i][2], ep[i][3]}) + ((f32x2){ep[i][4], ep[i][5]} + (f32x2){ep[i][6], ep[i][7]});
    const f32x2 b = ((f32x2){en[i][0], en[i][1]} + (f32x2){en[i][2], en[i][3]}) + ((f32x2){en[i][4], en[i][5]} + (f32x2){en[i][6], en[i][7]});
    sp[i] = a[0] + a[1]; sn[i] = b[0] + b[1]; }
  SCHED0();
#pragma unroll
  for (int i = 0; i < 2; ++i) { auto t1 = __builtin_amdgcn_permlane16_swap(__float_as_uint(sp[i]), __float_as_uint(sn[i]), false, false); m[i] = __uint_as_float(t1[0]) + __uint_as_float(t1[1]); }
#pragma unroll
  for (int i = 0; i < 2; ++i) { auto t2 = __builtin_amdgcn_permlane32_swap(__float_as_uint(m[i]), __float_as_uint(m[i]), false, false); m[i] = __builtin_amdgcn_rcpf(__uint_as_float(t2[0]) + __uint_as_float(t2[1])); }
#pragma unroll
  for (int i = 0; i < 2; ++i) { auto t3 = __builtin_amdgcn_permlane16_swap(__float_as_uint(m[i]), __float_as_uint(m[i]), false, false); sp[i] = __uint_as_float(t3[0]); sn[i] = __uint_as_float(t3[1]); }
  SCHED0();
#pragma unroll
  for (int i = 0; i < 2; ++i)
#pragma unroll
    for (int j = 0; j < 4; ++j) { pos[i][j] = cvtpk(ep[i][2 * j] * sp[i], ep[i][2 * j + 1] * sp[i]); neg[i][j] = cvtpk(en[i][2 * j] * sn[i], en[i][2 * j + 1] * sn[i]); }
}
DI unsigned swiglu2(f32x2 x, f32x2 u) {
  const f32x2 e = (f32x2){__builtin_amdgcn_exp2f(x[0]), __builtin_amdgcn_exp2f(x[1])} + 1.f, h = x * u * (f32x2){__builtin_amdgcn_rcpf(e[0]), __builtin_amdgcn_rcpf(e[1])};
  return cvtpk(h[0], h[1]);
}

constexpr int L_XS = 16384, L_META = 32768;
constexpr int L_WPH = 147456, L_MP3 = L_WPH + 4096, L_SV = L_MP3 + 8192;
constexpr int L_PH = 73728, L_VV = 32768, L_HO = L_PH + 32768, L_KVS = 131072, L_ZS = L_KVS + 8192;
constexpr int F_ABUF = 32768, F_BIMG = 14336, F_BBUF = 2 * F_BIMG, F_LDSB = 2 * F_ABUF;
constexpr int L_MODS = 161792, L_JUNK = L_MODS + 1280, L_CTR = L_JUNK + 256, LDS_TOTAL = L_CTR + 64;

struct Args { const u16 *x, *wqkv, *wphi, *wo, *w1, *w3, *w2, *c, *wmod; char* ws; u16* out; };

template <int N> DI void poll_granules(rsrc_t rWS, unsigned off, unsigned stride, unsigned off2, unsigned stride2, unsigned tag, u32x4 (&v)[N]) {
#pragma unroll 1
  for (unsigned spin = 0; spin < (1u << 20); ++spin) {
    bool ok = true;
#pragma unroll
    for (int i = 0; i < N; ++i) v[i] = ld16(rWS, i < 4 ? off + i * stride : off2 + (i - 4) * stride2);
#pragma unroll
    for (int i = 0; i < N; ++i) ok &= v[i][1] == tag && v[i][3] == tag;
    if (__all(ok)) break;
    asm volatile("s_sleep 1" ::: "memory");
  }
}
DI void stats_put(const u32x4 (&y)[2], rsrc_t rWS, unsigned st, unsigned ct, unsigned tag, int lane) {
  float v[16];
#pragma unroll
  for (int j = 0; j < 8; ++j) { v[2 * j] = bflo(y[j >> 2][j & 3]); v[2 * j + 1] = bfhi(y[j >> 2][j & 3]); }
  const float mw = xg_sum((((v[0] + v[1]) + (v[2] + v[3])) + ((v[4] + v[5]) + (v[6] + v[7]))) + (((v[8] + v[9]) + (v[10] + v[11])) + ((v[12] + v[13]) + (v[14] + v[15])))) * (1.f / 64.f);
  float t0 = 0, t1 = 0;
#pragma unroll
  for (int j = 0; j < 8; ++j) { const float d0 = v[2 * j] - mw, d1 = v[2 * j + 1] - mw; t0 += d0 * d0; t1 += d1 * d1; }
  const float qq = xg_sum(t0 + t1);
  if (lane < 16) st16(rWS, st + ct * 256 + lane * 16, (u32x4){__float_as_uint(mw), tag, __float_as_uint(qq), tag});
}
template <int N> DI f32x2 stats_fin(const u32x4 (&e)[N]) {
  const float mean = xg_sum((__uint_as_float(e[0][0]) + __uint_as_float(e[1][0])) + (__uint_as_float(e[2][0]) + __uint_as_float(e[3][0]))) * (1.f / 16.f);
  float q = 0;
#pragma unroll
  for (int i = 0; i < 4; ++i) { const float d = __uint_as_float(e[i][0]) - mean; q += __uint_as_float(e[i][2]) + 64.f * d * d; }
  return (f32x2){mean, rsqrtf(xg_sum(q) * (1.f / 1024.f) + 1e-5f)};
}
DI f32x4 lo4(u32x4 v) { return (f32x4){bflo(v[0]), bfhi(v[0]), bflo(v[1]), bfhi(v[1])}; }
DI f32x4 hi4(u32x4 v) { return (f32x4){bflo(v[2]), bfhi(v[2]), bflo(v[3]), bfhi(v[3])}; }
DI void ln_store(const u32x4 (&y)[2], f32x2 st, unsigned ldssh, unsigned ldssc, rsrc_t rWS, unsigned off) {
#pragma unroll
  for (int h = 0; h < 2; ++h)
    st16(rWS, off + h * 131072, pack8u((lo4(y[h]) - st[0]) * st[1] * LDSV(f32x4, ldssc + h * 128) + LDSV(f32x4, ldssh + h * 128), (hi4(y[h]) - st[0]) * st[1] * LDSV(f32x4, ldssc + h * 128 + 64) + LDSV(f32x4, ldssh + h * 128 + 64)));
}
DI void mod_panel(unsigned panel, const bf16x8 (&sf)[2], rsrc_t rWS, unsigned off, unsigned tag, int lane) {
  const int fr = lane & 15, g = lane >> 4, q = fr >> 2, p = lane & 3; const unsigned h = 2 * (g & 1) + (q >> 1);
  f32x4 r[4];
#pragma unroll
  for (int b = 0; b < 4; ++b) { const unsigned ad = panel + (4 * g + q) * 128 + ((b ^ h) << 5) + 8 * p;
    r[b] = MFMA16(cat8(lds_tr(ad), lds_tr(ad + 2048)), sf[0], ((f32x4){0.f, 0.f, 0.f, 0.f})); r[b] = MFMA16(cat8(lds_tr(ad + 4096), lds_tr(ad + 6144)), sf[1], r[b]); }
  const f32x4 v = {sel4(p, r[0][0], r[1][0], r[2][0], r[3][0]), sel4(p, r[0][1], r[1][1], r[2][1], r[3][1]), sel4(p, r[0][2], r[1][2], r[2][2], r[3][2]), sel4(p, r[0][3], r[1][3], r[2][3], r[3][3])};
  if (q < 2) st16(rWS, off + (p * 8 + 2 * g + q) * 16, (u32x4){__float_as_uint(q ? v[2] : v[0]), tag, __float_as_uint(q ? v[3] : v[1]), tag});
}

template <int ROLE>
DI void ffn1_wave(const Args& a, unsigned mt, unsigned nt, const unsigned (&voff)[7], const rsrc_t rS, unsigned sbase, unsigned ldsd, unsigned lds0, unsigned wr, int lane) {
  constexpr unsigned wc = ROLE, SSTEP = ROLE == 0 ? 64 * FD * 2 : 2 * 128 * 1024;
  auto issue = [&](unsigned ldsoff, rsrc_t r, unsigned so) {
    if (ROLE == 0) {
#pragma unroll
      for (int j = 0; j < 7; ++j) dma16<0>(ldsd + ldsoff + j * 1024, voff[j], r, so);
    } else {
#pragma unroll
      for (int i = 0; i < 8; ++i) dma16<1>(ldsd + ldsoff + i * 1024, voff[0], r, so + i * 1024);
    }
  };
  if (ROLE == 1) issue(0, rS, sbase);
  BAR();
  issue(ROLE == 0 ? F_BBUF : F_ABUF, rS, sbase + SSTEP);
  const int g = lane >> 4, q = (lane & 15) >> 2, p = lane & 3;
  const unsigned aaddr = lds0 + wr * 4096 + lane * 16;
  const unsigned baddr = lds0 + F_LDSB + (4 * g + q) * 224 + wc * 128 + 8 * p;
  const unsigned maddr = lds0 + F_LDSB + (4 * g + q) * 224 + 96 + wc * 16 + 8 * (p & 1) + (p >> 1) * F_BIMG;
  f32x4 acc[4][7];
#pragma unroll
  for (int m = 0; m < 4; ++m)
#pragma unroll
    for (int n = 0; n < 7; ++n) acc[m][n] = (f32x4){0.f, 0.f, 0.f, 0.f};
  bf16x8 a0[4], b0[7], a1[4], b1[7]; s16x4 tlo[7];
  unsigned ra = aaddr, rb = baddr, rm = maddr;
  auto rd = [&](int i, bf16x8(&av)[4], bf16x8(&b)[7], int s) {
    if (i < 14) { const int n = i >> 1; const unsigned ad = (n < 6 ? rb + (n % 3) * 32 + (n / 3) * F_BIMG : rm) + s * 32 * 224;
      if (i & 1) b[n] = cat8(tlo[n], lds_tr(ad + 16 * 224)); else tlo[n] = lds_tr(ad); }
    else av[i - 14] = lds_rd128(ra + s * 16384 + (i - 14) * 1024);
  };
  unsigned aoff = 0, boff = 0, poff = 0, so = sbase + (ROLE == 0 ? 2 : 1) * SSTEP;
  auto advance = [&]() { poff = ROLE == 0 ? boff : aoff; aoff ^= F_ABUF; boff = boff == 2 * F_BBUF ? 0 : boff + F_BBUF; so += SSTEP;
    ra = aaddr + aoff; rb = baddr + boff; rm = maddr + boff; asm volatile("" : "+v"(ra), "+v"(rb), "+v"(rm)); };
  auto step = [&](bf16x8(&ca)[4], bf16x8(&cb)[7], bf16x8(&na)[4], bf16x8(&nb)[7], int s, int barat, bool first) {
#pragma unroll
    for (int i = 0; i < 28; ++i) {
      if (i == barat - 2) { __builtin_amdgcn_s_setprio(2); SCHED0(); }
      if (i == barat) { WAITL0(); if (first) WAITV(0); else WAITV(1); SCHED0(); BAR(); SCHED0(); }
      if (i == 27 && barat > 27) { WAITL0(); SCHED0(); }
      acc[i / 7][i % 7] = MFMA16(cb[i % 7], ca[i / 7], acc[i / 7][i % 7]);
      SCHED0();
      if (i < 18) { rd(i, na, nb, s); SCHED0(); }
    }
    if (barat < 28) { __builtin_amdgcn_s_setprio(0); SCHED0(); }
  };
  WAITV(ROLE == 0 ? 7 : 8); BAR();
  if (ROLE == 0) __builtin_amdgcn_s_setprio(1);
#pragma unroll
  for (int i = 0; i < 18; ++i) rd(i, a0, b0, 0);
  SCHED0();
  if (ROLE == 0) {
    step(a0, b0, a1, b1, 1, 99, false);
    issue(2 * F_BBUF, rS, sbase + 2 * SSTEP); SCHED0();
#pragma unroll 1
    for (int t = 0; t < 15; ++t) {
      advance();
      if (t == 14) WAITV(0);
      WAITV(7); SCHED0(); BAR(); SCHED0();
      step(a1, b1, a0, b0, 0, 99, false);
      step(a0, b0, a1, b1, 1, 99, false);
      if (t < 13) { issue(poff, rS, so); SCHED0(); }
    }
  } else {
    step(a0, b0, a1, b1, 1, 25, true);
#pragma unroll 1
    for (int t = 0; t < 14; ++t) {
      advance();
      if (t >= 7 && t < 11) { u32x4 ones = {~0u, ~0u, ~0u, ~0u}; asm volatile("" : "+v"(ones));
        st16(mkrsrc(a.ws, WS_END), WS_XB + (blockIdx.x * 16 + (t - 7) * 4 + wr) * 1024 + lane * 16, ones); }
      issue(poff, rS, so);
      {
        constexpr unsigned NL = 7 * 2, NT13 = 12 / 2;
        const unsigned tt = t < NT13 - 1 ? t : NT13 - 1, ll = lane < NL ? lane : NL - 1, r = (ll * 37) >> 8;
        const char* tp = (const char*)((wr & 1) ? a.w3 : a.w1) + (size_t)((256 + (tt * 64 + (blockIdx.x >> 3) * 2 + (wr >> 1)) * 2 + r) * 56 + (blockIdx.x & 7) * 7 + ll - 7 * r) * 128;
        if (t == 13) {
          const unsigned e = ((blockIdx.x >> 3) * 4 + wr) * 56 + (lane < 56 ? lane : 55);
          touch((const char*)a.w2 + (size_t)(((blockIdx.x >> 2) & 1) * 1792 + (e >> 4) * 4 + (blockIdx.x & 3)) * 2048 + (e & 15) * 128, lds0 + L_JUNK); }
        else touch(tp, lds0 + L_JUNK); }
      SCHED0();
      step(a1, b1, a0, b0, 0, 99, false);
      step(a0, b0, a1, b1, 1, 25, false);
    }
    advance();
    touch((const char*)a.w2 + (size_t)(((blockIdx.x >> 2) & 1) * 3584 + (blockIdx.x >> 3) * 112 + wr * 28 + (lane < 28 ? lane : 27)) * 1024 + ((3 - ((uintptr_t)a.w2 >> 7)) & 7) * 128, lds0 + L_JUNK);
    SCHED0();
    step(a1, b1, a0, b0, 0, 99, false);
    step(a0, b0, a1, b1, 1, 99, false);
  }
  SCHED0();
#pragma unroll
  for (int i = 0; i < 28; ++i) acc[i / 7][i % 7] = MFMA16(b1[i % 7], a1[i / 7], acc[i / 7][i % 7]);
  SCHED0();
  SCHED0();
  const rsrc_t rH = mkrsrc(a.ws + WS_HH, 18 << 20);
  constexpr unsigned SLOT = 10240;
  const unsigned hb = (mt * 2 + (wr >> 1)) * SLOT, ho = (wr & 1) * 4096 + (lane & 15) * 16;
  auto hst = [&](unsigned B, unsigned par, unsigned o, u32x4 v) { o += ho + par * 512; __builtin_amdgcn_raw_buffer_store_b128(v, rH, skipi(o), hb + (B >> 1) * 16 * SLOT, 16); asm volatile("s_nop 1" ::"v"(v) : "memory"); };
  const unsigned par0 = nt & 1;
  u32x2 mx[2];
#pragma unroll
  for (int mp = 0; mp < 2; ++mp) {
#pragma unroll
    for (int n = 0; n < 3; ++n) {
      const unsigned B = 7 * nt + 4 * wc + n;
      u32x2 w[2];
#pragma unroll
      for (int i = 0; i < 2; ++i) { const f32x4 G = acc[2 * mp + i][n], U = acc[2 * mp + i][3 + n]; w[i] = (u32x2){swiglu2((f32x2){G[0], G[1]}, (f32x2){U[0], U[1]}), swiglu2((f32x2){G[2], G[3]}, (f32x2){U[2], U[3]})}; }
      hst(B, par0 ^ (n & 1), (2 * mp + (g & 1)) * 1024 + (g >> 1) * 256, widen(w[0], w[1]));
    }
    float mg[4], mu[4];
#pragma unroll
    for (int e = 0; e < 4; ++e) { auto r = __builtin_amdgcn_permlane32_swap(__float_as_uint(acc[2 * mp][6][e]), __float_as_uint(acc[2 * mp + 1][6][e]), false, false); mg[e] = __uint_as_float(r[0]); mu[e] = __uint_as_float(r[1]); }
    mx[mp] = (u32x2){swiglu2((f32x2){mg[0], mg[1]}, (f32x2){mu[0], mu[1]}), swiglu2((f32x2){mg[2], mg[3]}, (f32x2){mu[2], mu[3]})};
  }
  { const unsigned B = 7 * nt + 3; hst(B, par0 ^ 1, (2 * (g & 1) + (g >> 1)) * 1024 + wc * 256, widen(mx[0], mx[1])); }
  SCHED0();
  __builtin_amdgcn_s_setprio(0);
}

__global__ __launch_bounds__(512, 2) void k_main(const Args a) {
  extern __shared__ __attribute__((aligned(16))) char smem[];
  const int tid = threadIdx.x, lane = tid & 63;
  const unsigned wid = rfl(tid >> 6), lds0 = (unsigned)(uintptr_t)(LAS char*)smem, w4 = wid & 3;
  const bool isL = wid >= 4;
  const unsigned bid = blockIdx.x, xcd = bid & 7, loc = bid >> 3;
  const int fr = lane & 15, g = lane >> 4, q = fr >> 2, p = lane & 3;
  const unsigned wm = w4 & 1, wn = w4 >> 1;
  if (wid == 6) {
    u64 pc; unsigned len;
    asm volatile("s_getpc_b64 %0\n.Lcode_ref:\n\ts_mov_b32 %1, .Lcode_end-.Lcode_ref" : "=s"(pc), "=s"(len));
    const unsigned o = ((lane < 13 ? lane : 12) * 32 + loc) * 128;
    touch((const char*)pc + (o < len ? o : 0), lds0 + L_JUNK);
  }
  asm volatile("" ::"s"(a.x), "s"(a.ws), "s"(a.out));
  u64* const flg = (u64*)(a.ws + WS_CNT);
  const rsrc_t rWS = mkrsrc(a.ws, WS_END);
  const unsigned mtq = 8 * (xcd & 1) + (loc & 7), hp = 4 * (xcd >> 1) + (loc >> 3);
  const unsigned mto = 4 * (xcd & 3) + (loc & 3), cto = 2 * (loc >> 2) + (xcd >> 2);
  const unsigned mtf = 4 * (xcd & 1) + (loc & 3), ntf = 8 * (xcd >> 1) + (loc >> 2);
  const unsigned ks2 = cto & 1;
  const unsigned mbl = 2 * w4 + (wid >> 2), mbo = mto * 8 + mbl;
  const rsrc_t rM = mkrsrc(a.wmod, DD * 6 * DD * 2);
  const unsigned voffM = (lane >> 3) * (6 * DD * 2) + (((lane & 7) ^ (((lane >> 4) & 3) << 1)) << 4), ctm = cto ^ (w4 + 1)  , soM = mto * 64 * (6 * DD * 2) + ctm * 128;
  { const void* mp = lane < 32 ? (const void*)(a.c + mto * 64 + lane * 2) : (const void*)((const unsigned*)(flg + F_EPOCH * FSTR) + (lane & 1));
    asm volatile("s_mov_b32 m0, %1\n\ts_nop 0\n\tglobal_load_lds_dword %0, off sc1" ::"v"(mp), "s"(lds0 + L_META + wid * 256) : "memory"); }
  const bool isM = wid == 4 || wid == 5;
  if (isM) {
#pragma unroll
    for (int i = 0; i < 8; ++i) dma16<2>(lds0 + w4 * 8192 + i * 1024, voffM, rM, soM + i * 8 * (6 * DD * 2) + w4 * DD * 2);
  } else {
#pragma unroll
    for (int i = 0; i < 4; ++i) if (i < 2 || wid < 2) dma16<2>(lds0 + L_XS + mbl * 2048 + i * 1024, (lane >> 3) * (DD * 2) + (((lane & 7) ^ (lane >> 3)) << 4), mkrsrc(a.x, SD * DD * 2), (mbo * 16 + i * 8) * (DD * 2) + cto * 128);
  }
  if (tid < 16) LDSV(unsigned, lds0 + L_CTR + tid * 4) = 0u;
  WAITL0(); BAR();
  if (wid == 6) {
#pragma unroll
    for (int i = 0; i < 4; ++i) dma16<0>(lds0 + L_WPH + i * 1024, lane * 16, mkrsrc(a.wphi, 65536), hp * 4096 + i * 1024);
    WAITV(4);
  } else if (wid == 7) {
    const unsigned c3 = (3 - ((uintptr_t)a.wqkv >> 7)) & 7;
#pragma unroll
    for (int i = 0; i < 3; ++i) {
      const unsigned f = ((i * 2 + (xcd & 1)) * 32 + loc) * 64 + lane, row = (f * 43691u) >> 19, r = f - row * 12, cl = (xcd >> 1) * 4 + (r & 3);
      touch((const char*)a.wqkv + (size_t)row * 6144 + (r >> 2) * 2048 + (((cl & 7) == c3) ? cl ^ 1 : cl) * 128, lds0 + L_JUNK);
    }
    { const unsigned e = (lane < 24 ? lane : 23) * 256 + bid; touch((const char*)a.wqkv + (size_t)e * 1024 + c3 * 128, lds0 + L_JUNK); }
    WAITV(4);
  } else if (isM) { WAITV(8); } else { WAITV(0); }
  u64 tok;
  { const u32x2 e = LDSV(u32x2, lds0 + L_META + wid * 256 + 128); u64 z = (((u64)rfl(e[1]) << 32) | rfl(e[0])) + 0x9E3779B97F4A7C15ull; z = (z ^ (z >> 30)) * 0xBF58476D1CE4E5B9ull; tok = z ^ (z >> 27); }
  const unsigned tag = (unsigned)(tok >> 16);
  u32x4 y[2];
  auto read_x = [&](unsigned mb) {
#pragma unroll
    for (int h = 0; h < 2; ++h) { const unsigned xa = lds0 + L_XS + mb * 2048 + fr * 128 + (g & 1) * 8;
      const u32x2 lo = LDSV(u32x2, xa + (((4 * h + (g >> 1)) ^ (fr & 7)) << 4)), hi = LDSV(u32x2, xa + (((4 * h + 2 + (g >> 1)) ^ (fr & 7)) << 4)); y[h] = (u32x4){lo[0], lo[1], hi[0], hi[1]}; }
  };
  if (!isM) {
#pragma unroll 1
    for (int r = wid < 2 ? 1 : 0; r >= 0; --r) { read_x(mbl + r); stats_put(y, rWS, WS_ST1 + (mbo + r) * 4096, cto, tag, lane); }
  }
  bf16x8 sf[2];
  if (isL) {
    const float v = bflo(LDSV(u16, lds0 + L_META + wid * 256 + lane * 2)); LDSV(u16, lds0 + L_SV + 512 + w4 * 128 + lane * 2) = (u16)cvtpk(v * __builtin_amdgcn_rcpf(1.f + __expf(-v)), 0.f);
#pragma unroll
    for (int s = 0; s < 2; ++s) { const u32x2 lo = LDSV(u32x2, lds0 + L_SV + 512 + w4 * 128 + s * 64 + g * 8), hi = LDSV(u32x2, lds0 + L_SV + 512 + w4 * 128 + s * 64 + 32 + g * 8);
      sf[s] = __builtin_bit_cast(bf16x8, ((u32x4){lo[0], lo[1], hi[0], hi[1]})); }
    if (isM) { WAITV(0); mod_panel(lds0 + w4 * 8192, sf, rWS, WS_MODT + ((w4 * 16 + ctm) * 16 + mto) * 512, tag, lane); }
  }
  WAITV(0);
  {
    u32x4 e[6];
    poll_granules<6>(rWS, WS_ST1 + mbo * 4096 + g * 1024 + fr * 16, 256, WS_MODT + (((wid >> 2) * 16 + cto) * 16 + (lane >> 3)) * 512 + (w4 * 8 + (lane & 7)) * 16, 8 * 512, tag, e);
    float m0 = __uint_as_float(e[4][0]) + __uint_as_float(e[5][0]), m1 = __uint_as_float(e[4][2]) + __uint_as_float(e[5][2]);
    m0 = rbf(xg_sum(m0 + ror8(m0))); m1 = rbf(xg_sum(m1 + ror8(m1)));
    if (isL) { m0 = rbf(1.f + m0); m1 = rbf(1.f + m1); }
    if (lane < 8) LDSV(f32x2, lds0 + L_SV + wid * 64 + lane * 8) = (f32x2){m0, m1};
    const f32x2 st = stats_fin(e);
    WAITL0(); BAR();
    if (isM) read_x(mbl);
    ln_store(y, st, lds0 + L_SV + g * 16, lds0 + L_SV + 256 + g * 16, rWS, WS_H1 + (cto * 2 * 128 + mbo) * 1024 + lane * 16);
    asm volatile("" : "+v"(y[0]), "+v"(y[1]));
    publish<8>(lds0 + L_CTR, flg + (F_H1 + bid) * FSTR, tok, lane);
  }
  bf16x8 qf[2], kf2[2], wf[2];
  if (isL) {
    Loader<3, 3, 4, 0, 49152, false, 0, false> ld; ld.init(a.ws + WS_H1, SD * DD * 2, a.wqkv, DD * 3 * DD * 2, 3 * DD, hp * 64, DD, mtq, 0, lds0, w4, lane);
    ld.preW<3>();
    BAR();
    ld.run<16, 3, 9>([&]() {
#pragma unroll
      for (int i = 0; i < 8; ++i) dma16<2>(lds0 + (w4 < 3 ? 49152 + w4 * 8192 : L_MP3) + i * 1024, voffM, rM, soM + i * 8 * (6 * DD * 2) + (2 + w4) * DD * 2);
      { const unsigned e = loc * 256 + (xcd & 3) * 64 + w4 * 16 + (lane & 15); touch((const char*)a.wo + (size_t)e * 256 + (xcd >> 2) * 128, lds0 + L_JUNK); }
    });
    BAR();
    { const unsigned l28 = lane < 28 ? lane : 27, r4 = (l28 * 37) >> 8; touch((const char*)((w4 & 1) ? a.w3 : a.w1) + (size_t)((loc * 8 + (w4 >> 1) * 4 + r4) * 56 + xcd * 7 + l28 - 7 * r4) * 128, lds0 + L_JUNK); }
    { const unsigned e = loc * 256 + w4 * 64 + lane; touch((const char*)a.wo + (size_t)e * 256 + (xcd >> 2) * 128, lds0 + L_JUNK); }
#pragma unroll
    for (int i = 0; i < 2; ++i) { kf2[i] = lds_rd128(lds0 + L_HO + (w4 * 4 + i) * 1024 + lane * 16); qf[i] = lds_rd128(lds0 + L_HO + (w4 * 4 + 2 + i) * 1024 + lane * 16); }
  } else {
    { const unsigned c3 = (3 - ((uintptr_t)a.wqkv >> 7)) & 7;
      if (wid != 0 && (c3 >> 2) == ((xcd >> 1) & 1)) touch((const char*)a.wqkv + (size_t)(256 + loc * 24 + (lane < 24 ? lane : 23)) * 6144 + (wid - 1) * 2048 + (xcd >> 2) * 1024 + c3 * 128, lds0 + L_JUNK); }
    if (wid == 0) wait_flags(flg + (F_H1 + bid_wo(mtq, fr)) * FSTR, tok);
    BAR();
    f32x4 acc[4][6];
    g128_mma<3, 3, 4, 0, 49152, false, 0>(16, lds0, w4, lane, acc);
#pragma unroll
    for (int i = 0; i < 2; ++i) {
      LDSV(u32x4, lds0 + L_HO + (w4 * 4 + i) * 1024 + lane * 16) = pack8u(acc[2 + i][2], acc[2 + i][3]);
      LDSV(u32x4, lds0 + L_HO + (w4 * 4 + 2 + i) * 1024 + lane * 16) = pack8u(acc[2 + i][0], acc[2 + i][1]);
    }
    WAITL0(); BAR();
    { const unsigned va = lds0 + L_VV + wn * 8192 + (wm * 64 + fr) * 64 + ((g ^ (((fr >> 1) & 1) | ((fr >> 3) << 1))) << 3), sw = (fr >> 2) & 1;
#pragma unroll
      for (int m = 0; m < 4; ++m)
#pragma unroll
        for (int db = 0; db < 2; ++db) { const f32x4 v = acc[m][4 + db]; LDSV(u32x2, va + m * 1024 + ((db ^ sw) << 5)) = (u32x2){cvtpk(v[0], v[1]), cvtpk(v[2], v[3])}; } }
#pragma unroll
    for (int i = 0; i < 2; ++i) { kf2[i] = pack8(acc[i][2], acc[i][3]); qf[i] = pack8(acc[i][0], acc[i][1]); }
  }
  {
#pragma unroll
    for (int fb = 0; fb < 2; ++fb) { const unsigned ad = lds0 + L_WPH + wn * 2048 + (4 * g + q) * 64 + fb * 32 + 8 * p; wf[fb] = cat8(lds_tr(ad), lds_tr(ad + 1024)); }
    const unsigned mloc = wm * 4 + (isL ? 2 : 0);
    {
      u32x4 pos[2], neg[2];
      feature_map2(kf2, wf, pos, neg);
      const unsigned pa = lds0 + L_PH + wn * 16384 + (mloc * 16 + fr) * 128 + ((g ^ ((fr & 1) | ((fr >> 3) << 1))) << 3), sw = (fr >> 1) & 3;
#pragma unroll
      for (int i = 0; i < 2; ++i) {
        LDSV(u32x2, pa + i * 2048 + ((0 ^ sw) << 5)) = (u32x2){pos[i][0], pos[i][1]}; LDSV(u32x2, pa + i * 2048 + ((1 ^ sw) << 5)) = (u32x2){pos[i][2], pos[i][3]};
        LDSV(u32x2, pa + i * 2048 + ((2 ^ sw) << 5)) = (u32x2){neg[i][0], neg[i][1]}; LDSV(u32x2, pa + i * 2048 + ((3 ^ sw) << 5)) = (u32x2){neg[i][2], neg[i][3]};
      }
    }
    WAITL0(); BAR();
    {
      const unsigned hh = wid >> 2, rr = 4 * g + q;
      const unsigned pa = lds0 + L_PH + hh * 16384 + rr * 128 + ((w4 ^ ((rr >> 1) & 3)) << 5) + ((p ^ ((rr & 1) | ((rr >> 3) << 1))) << 3);
      const unsigned va = lds0 + L_VV + hh * 8192 + rr * 64 + ((p ^ (((rr >> 1) & 1) | ((rr >> 3) << 1))) << 3), vsw = (rr >> 2) & 1;
      f32x4 kv[3] = {{0.f, 0.f, 0.f, 0.f}, {0.f, 0.f, 0.f, 0.f}, {0.f, 0.f, 0.f, 0.f}};
      const bf16x8 ones = __builtin_bit_cast(bf16x8, ((u32x4){0x3f803f80u, 0x3f803f80u, 0x3f803f80u, 0x3f803f80u}));
#pragma unroll
      for (int ks = 0; ks < 4; ++ks) {
        const bf16x8 pf = cat8(lds_tr(pa + ks * 4096), lds_tr(pa + ks * 4096 + 2048));
#pragma unroll
        for (int db = 0; db < 2; ++db) { const unsigned ad = va + ks * 2048 + ((db ^ vsw) << 5); kv[db] = MFMA16(pf, cat8(lds_tr(ad), lds_tr(ad + 1024)), kv[db]); }
        kv[2] = MFMA16(pf, ones, kv[2]);
      }
      const unsigned dst = WS_KVP + (hp * 16 + mtq) * KVPB;
      st16(rWS, dst + tid * 16, pack8u(kv[0], kv[1]));
      if (fr == 0) st16(rWS, dst + 8192 + wid * 64 + g * 16, __builtin_bit_cast(u32x4, kv[2]));
    }
    publish<8>(lds0 + L_CTR + 4, flg + (F_KV + bid) * FSTR, tok, lane);
    {
      const rsrc_t rW = mkrsrc(a.wo, DD * DD * 2);
      const unsigned voffW = (lane >> 3) * (DD * 2) + (((lane & 7) ^ (((lane >> 4) & 3) << 1)) << 4);
#pragma unroll
      for (int i = 0; i < 7; ++i) { const unsigned pc = (i < 4 ? i : i + 9) * 8 + wid; dma16<0>(lds0 + pc * 1024, voffW, rW, pc * 8 * (DD * 2) + cto * 128); }
    }
    if (isL) mod_panel(lds0 + (w4 < 3 ? 49152 + w4 * 8192 : L_MP3), sf, rWS, WS_MODT + (((2 + w4) * 16 + ctm) * 16 + mto) * 512, tag, lane);
    u32x4 php[2], phn[2];
    feature_map2(qf, wf, php, phn);
    if (wid == 0) wait_flags(flg + (F_KV + bid_qkv(fr, hp)) * FSTR, tok);
    BAR();
    {
      u32x4 v[16];
#pragma unroll
      for (int i = 0; i < 16; ++i) v[i] = ld16(rWS, WS_KVP + (hp * 16 + i) * KVPB + tid * 16);
      float zv[4];
#pragma unroll
      for (int i = 0; i < 4; ++i) zv[i] = __uint_as_float(__builtin_amdgcn_raw_buffer_load_b32(rWS, WS_KVP + (hp * 16 + 4 * g + i) * KVPB + 8192 + (wid * 16 + fr) * 4, 0, 16));
      float s[8] = {0, 0, 0, 0, 0, 0, 0, 0};
      unsigned elo = 0x00003f80u, ehi = 0x3f800000u; asm volatile("" : "+v"(elo), "+v"(ehi));
#pragma unroll
      for (int i = 0; i < 16; ++i)
#pragma unroll
        for (int j = 0; j < 4; ++j) { s[2 * j] = dot2(v[i][j], elo, s[2 * j]); s[2 * j + 1] = dot2(v[i][j], ehi, s[2 * j + 1]); }
      LDSV(u32x4, lds0 + L_KVS + tid * 16) = (u32x4){cvtpk(s[0], s[1]), cvtpk(s[2], s[3]), cvtpk(s[4], s[5]), cvtpk(s[6], s[7])};
      const float z = xg_sum((zv[0] + zv[1]) + (zv[2] + zv[3]));
      if (g == 0) LDSV(u16, lds0 + L_ZS + (wid * 16 + fr) * 2) = (u16)cvtpk(z, z);
    }
    WAITL0(); BAR();
    bf16x8 kf[2][3];
#pragma unroll
    for (int ks = 0; ks < 2; ++ks) {
      const u32x4 u0 = LDSV(u32x4, lds0 + L_KVS + (wn * 4 + 2 * ks) * 1024 + lane * 16), u1 = LDSV(u32x4, lds0 + L_KVS + (wn * 4 + 2 * ks + 1) * 1024 + lane * 16);
      kf[ks][0] = __builtin_bit_cast(bf16x8, ((u32x4){u0[0], u0[1], u1[0], u1[1]})); kf[ks][1] = __builtin_bit_cast(bf16x8, ((u32x4){u0[2], u0[3], u1[2], u1[3]}));
      const u32x2 za = LDSV(u32x2, lds0 + L_ZS + wn * 128 + ks * 64 + g * 8), zb = LDSV(u32x2, lds0 + L_ZS + wn * 128 + ks * 64 + 32 + g * 8);
      kf[ks][2] = __builtin_bit_cast(bf16x8, ((u32x4){za[0], za[1], zb[0], zb[1]}));
    }
#pragma unroll
    for (int i = 0; i < 2; ++i) {
      f32x4 o[3];
#pragma unroll
      for (int db = 0; db < 3; ++db) { o[db] = MFMA16(kf[0][db], __builtin_bit_cast(bf16x8, php[i]), ((f32x4){0.f, 0.f, 0.f, 0.f})); o[db] = MFMA16(kf[1][db], __builtin_bit_cast(bf16x8, phn[i]), o[db]); }
      const float inv = __builtin_amdgcn_rcpf(o[2][0] + 1e-6f);
      st16(rWS, WS_OF + ((2 * hp + wn) * 128 + mtq * 8 + mloc + i) * 1024 + lane * 16, pack8u(o[0] * inv, o[1] * inv));
    }
    publish<8>(lds0 + L_CTR + 8, flg + (F_O + bid) * FSTR, tok, lane);
  }
  if (isL) {
    const rsrc_t rW = mkrsrc(a.wo, DD * DD * 2);
    const unsigned voffW = (lane >> 3) * (DD * 2) + (((lane & 7) ^ (((lane >> 4) & 3) << 1)) << 4);
#pragma unroll 2
    for (int i = 8; i < 26; ++i) dma16<0>(lds0 + (i * 4 + w4) * 1024, voffW, rW, (i * 4 + w4) * 8 * (DD * 2) + cto * 128);
    WAITV(0);
  } else {
    { u32x4 e[8];
      const unsigned mo = WS_MODT + (((2 + w4) * 16 + cto) * 16) * 512 + lane * 16;
      poll_granules<8>(rWS, mo, 1024, mo + 4096, 1024, tag, e);
      float m0 = 0, m1 = 0;
#pragma unroll
      for (int i = 0; i < 8; ++i) { m0 += __uint_as_float(e[i][0]); m1 += __uint_as_float(e[i][2]); }
      auto t0 = __builtin_amdgcn_permlane32_swap(__float_as_uint(m0), __float_as_uint(m1), false, false);
      float mv = rbf(__uint_as_float(t0[0]) + __uint_as_float(t0[1])); if (w4 == 2) mv = rbf(1.f + mv);
      mv *= w4 == 0 ? 1.f : w4 == 3 ? 0.4804530139182014f : -1.4426950408889634f;
      LDSV(float, lds0 + L_MODS + w4 * 256 + (lane & 31) * 8 + (lane >> 5) * 4) = mv; }
    if (wid == 0) wait_flags(flg + (F_O + bid_qkv(mto, fr)) * FSTR, tok);
    if (bid == 0 && tid == 0) __hip_atomic_store((gu64*)(flg + F_EPOCH * FSTR), tok, RLX_AGENT);
    WAITL0();
  }
  BAR();
  {
    constexpr int PD = 3;
    u32x4 af[16][2]; bf16x8 bf[2][4];
    const unsigned kh = wid >> 2, ao = WS_OF + (kh * 16 * 128 + mbo) * 1024 + lane * 16, h = 2 * (g & 1) + (q >> 1);
    unsigned ba[4];
#pragma unroll
    for (int n = 0; n < 4; ++n) { ba[n] = lds0 + kh * 65536 + (4 * g + q) * 128 + ((n ^ h) << 5) + 8 * p; asm volatile("" : "+v"(ba[n])); }
#pragma unroll
    for (int ks = 0; ks < PD; ++ks) { af[ks][0] = __builtin_amdgcn_raw_buffer_load_b128(rWS, ao, ks * 131072, 16); af[ks][1] = __builtin_amdgcn_raw_buffer_load_b128(rWS, ao ^ 1024, ks * 131072, 16); }
#pragma unroll
    for (int n = 0; n < 4; ++n) bf[0][n] = cat8(lds_tr(ba[n]), lds_tr(ba[n] + 2048));
    SCHED0();
    f32x4 acc[4], oth[4];
#pragma unroll
    for (int n = 0; n < 4; ++n) { acc[n] = (f32x4){0.f, 0.f, 0.f, 0.f}; oth[n] = acc[n]; }
#pragma unroll
    for (int ks = 0; ks < 16; ++ks) {
      if (ks + PD < 16) { af[ks + PD][0] = __builtin_amdgcn_raw_buffer_load_b128(rWS, ao, (ks + PD) * 131072, 16); af[ks + PD][1] = __builtin_amdgcn_raw_buffer_load_b128(rWS, ao ^ 1024, (ks + PD) * 131072, 16); }
      if (ks + 1 < 16) {
#pragma unroll
        for (int n = 0; n < 4; ++n) { const unsigned ad = ba[n] + (ks + 1) * 4096; bf[(ks + 1) & 1][n] = cat8(lds_tr(ad), lds_tr(ad + 2048)); }
      }
      if (ks == 15) { WAITL0(); BAR(); }
      SCHED0();
#pragma unroll
      for (int n = 0; n < 4; ++n) oth[n] = MFMA16(bf[ks & 1][n], __builtin_bit_cast(bf16x8, af[ks][1]), oth[n]);
#pragma unroll
      for (int n = 0; n < 4; ++n) acc[n] = MFMA16(bf[ks & 1][n], __builtin_bit_cast(bf16x8, af[ks][0]), acc[n]);
      SCHED0();
    }
#pragma unroll
    for (int n = 0; n < 4; ++n) LDSV(f32x4, lds0 + (wid * 4 + n) * 1024 + lane * 16) = oth[n];
    WAITL0(); BAR();
#pragma unroll
    for (int n = 0; n < 4; ++n) acc[n] += LDSV(f32x4, lds0 + ((wid ^ 4) * 4 + n) * 1024 + lane * 16);
#pragma unroll
    for (int hh = 0; hh < 2; ++hh) y[hh] = pack8u(lo4(y[hh]) + LDSV(f32x4, lds0 + L_MODS + hh * 128 + g * 16) * acc[2 * hh], hi4(y[hh]) + LDSV(f32x4, lds0 + L_MODS + hh * 128 + 64 + g * 16) * acc[2 * hh + 1]);
    stats_put(y, rWS, WS_STAT + mbo * 4096, cto, tag, lane);
    WAITV(0);
    u32x4 e[4];
    poll_granules<4>(rWS, WS_STAT + mbo * 4096 + g * 1024 + fr * 16, 256, 0, 0, tag, e);
    const f32x2 st = stats_fin(e);
    ln_store(y, st, lds0 + L_MODS + 256 + g * 16, lds0 + L_MODS + 512 + g * 16, rWS, WS_H2 + (cto * 2 * 128 + mbo) * 1024 + lane * 16);
    asm volatile("" : "+v"(y[0]), "+v"(y[1]));
    publish<8>(lds0 + L_CTR + 16, flg + (F_Y + bid) * FSTR, tok, lane);
  }
  if (isL) {
    unsigned voff[7];
#pragma unroll
    for (int j = 0; j < 7; ++j) { const unsigned i = j * 64 + lane, k = (i * 4682u) >> 16, c = i - k * 14; voff[j] = k * (FD * 2) + c * 16; }
    const u16* src = w4 < 2 ? a.w1 : a.w3;
    const rsrc_t rS = mkrsrc(src, DD * FD * 2);
    const unsigned sbase = ntf * 224 + (w4 & 1) * 32 * (FD * 2), ldsd = lds0 + F_LDSB + w4 * 7 * 1024;
#pragma unroll
    for (int j = 0; j < 7; ++j) dma16<0>(ldsd + j * 1024, voff[j], rS, sbase);
    BAR();
    ffn1_wave<0>(a, mtf, ntf, voff, rS, sbase, ldsd, lds0, w4, lane);
  } else {
    if (wid == 0) wait_flags(flg + (F_Y + bid_wo(2 * mtf + (g & 1), fr)) * FSTR, tok);
    BAR();
    unsigned voff[7] = {(unsigned)lane * 16, 0, 0, 0, 0, 0, 0};
    const rsrc_t rS = mkrsrc(a.ws + WS_H2, SD * DD * 2);
    ffn1_wave<1>(a, mtf, ntf, voff, rS, ((w4 >> 1) * 128 + mtf * 16 + (w4 & 1) * 8) * 1024, lds0 + w4 * 8192, lds0, w4, lane);
  }
  publish<8>(lds0 + L_CTR + 20, flg + (F_H + bid) * FSTR, tok, lane);
  BAR();
  if (isL) {
    Loader<2, 4, 5, 0, 65536, true, 1, true> ld; ld.init(a.ws + WS_HH, 18 << 20, a.w2, FD * DD * 2, DD, cto * 64, ks2 ? -64 : 64, mto, ks2 * 28, lds0, w4, lane);
    ld.preW<5>();
    BAR();
    ld.run<28, 5, 0>([]() {});
  } else {
    if (wid == 0) wait_flags(flg + (F_H + (lane < 16 ? bid_f1(mto >> 1, ks2 * 16 + fr) : bid ^ 4)) * FSTR, tok);
    BAR();
    f32x4 acc[4][4];
    g128_mma<2, 4, 5, 0, 65536, true, 1>(28, lds0, w4, lane, acc);
#pragma unroll
    for (int m = 3; m >= 0; --m) st16(rWS, WS_XB + ((bid ^ 4) * 16 + wm * 8 + wn + 2 * m) * 1024 + lane * 16, __builtin_elementwise_min(pack8u(acc[m][2], acc[m][3]), (u32x4){~1u, ~1u, ~1u, ~1u}));
#pragma unroll
    for (int i = 0; i < 8; ++i) LDSV(f32x4, lds0 + ((wm * 4 + (i >> 1)) * 4 + wn * 2 + (i & 1)) * 1024 + lane * 16) = acc[i >> 1][i & 1];
    WAITV(0); WAITL0();
  }
  BAR();
  {
    f32x4 gt[4], s[4];
    u32x4 o[2];
#pragma unroll
    for (int i = 0; i < 2; ++i) o[i] = ld16(rWS, WS_XB + (bid * 16 + mbl * 2 + i) * 1024 + lane * 16);
#pragma unroll
    for (int n = 0; n < 4; ++n) { gt[n] = LDSV(f32x4, lds0 + L_MODS + 768 + n * 64 + g * 16); s[n] = ((n & 1) ? hi4(y[n >> 1]) : lo4(y[n >> 1])) + gt[n] * LDSV(f32x4, lds0 + (mbl * 4 + n) * 1024 + lane * 16); }
#pragma unroll 1
    for (unsigned spin = 0; spin < (1u << 20); ++spin) {
      const u32x4 mx = __builtin_elementwise_max(o[0], o[1]);
      if (__all(max(max(mx[0], mx[1]), max(mx[2], mx[3])) != ~0u)) break;
      asm volatile("s_sleep 1" ::: "memory");
#pragma unroll
      for (int i = 0; i < 2; ++i) o[i] = ld16(rWS, WS_XB + (bid * 16 + mbl * 2 + i) * 1024 + lane * 16);
    }
#pragma unroll
    for (int n = 0; n < 4; ++n) s[n] += gt[n] * ((n & 1) ? hi4(o[n >> 1]) : lo4(o[n >> 1]));
    u32x4 w[2];
#pragma unroll
    for (int h = 0; h < 2; ++h) w[h] = widen((u32x2){cvtpk(s[2 * h][0], s[2 * h][1]), cvtpk(s[2 * h][2], s[2 * h][3])}, (u32x2){cvtpk(s[2 * h + 1][0], s[2 * h + 1][1]), cvtpk(s[2 * h + 1][2], s[2 * h + 1][3])});
    const rsrc_t rO = mkrsrc(a.out, SD * DD * 2);
    {
      const bool up = fr >= 8; u32x4 t;
#pragma unroll
      for (int k = 0; k < 4; ++k) t[k] = __float_as_uint(ror8(__uint_as_float(up ? w[0][k] : w[1][k])));
      const unsigned o = ((mbo * 16 + (fr & 7)) * DD + cto * 64 + (fr >> 3) * 32 + (g & 1) * 16 + (g >> 1) * 8) * 2;
      __builtin_amdgcn_raw_buffer_store_b128(up ? t : w[0], rO, o, 0, 18);
      __builtin_amdgcn_raw_buffer_store_b128(up ? w[1] : t, rO, o + 8 * DD * 2, 0, 18);
    }
    asm volatile(".Lcode_end:");
  }
}

extern "C" void kernel_launch(void* const* d_in, const int* in_sizes, int n_in, void* d_out, int out_size, void* d_ws, size_t ws_size, hipStream_t stream) {
  Args a;
  a.x = (const u16*)d_in[0]; a.wqkv = (const u16*)d_in[1]; a.wphi = (const u16*)d_in[2]; a.wo = (const u16*)d_in[3]; a.w1 = (const u16*)d_in[5]; a.w3 = (const u16*)d_in[6]; a.w2 = (const u16*)d_in[7];
  a.c = (const u16*)d_in[8]; a.wmod = (const u16*)d_in[9]; a.ws = (char*)d_ws + (ws_size >= ((size_t)256 << 20) ? (size_t)192 << 20 : 0); a.out = (u16*)d_out;
  (void)hipFuncSetAttribute((const void*)k_main, hipFuncAttributeMaxDynamicSharedMemorySize, LDS_TOTAL);
  k_main<<<256, 512, LDS_TOTAL, stream>>>(a);
}
```
